```python
import torch

def _softmax(x, axis=-1):
    return torch.softmax(x, dim=axis)
def _gelu(x):
    return torch.nn.functional.gelu(x, approximate="tanh")
def _silu(x):
    return torch.nn.functional.silu(x)
def _sigmoid(x):
    return torch.sigmoid(x)
def _layernorm(x, axis=-1, eps=1e-5):
    m = x.mean(dim=axis, keepdim=True)
    v = x.var(dim=axis, keepdim=True, unbiased=False)
    return (x - m) / torch.sqrt(v + eps)
def _scatter_add(tgt, idx, src, axis=0):
    return tgt.clone().index_add_(axis, idx, src)
def _segment_sum(src, seg, n):
    out = torch.zeros((n,) + src.shape[1:], dtype=src.dtype)
    return out.index_add_(0, seg, src)
def _pad(x, pw):
    flat = tuple(v for pair in reversed(pw) for v in pair)
    return torch.nn.functional.pad(x, flat)

kernel_name = 'standalone__gqa_softmax__near_ridge'

def setup_inputs():

    _w = (lambda a: (lambda t: t.to(torch.bfloat16) if t.is_floating_point() else t)(torch.as_tensor(a)))
    B, Hq, Hkv, Sq, Sk, D = 8, 32, 8, 144, 1024, 128
    return {
        "q": _w((lambda *s: torch.randn(*s, dtype=torch.bfloat16) * 0.8)(B, Hq, Sq, D)),
        "k": _w((lambda *s: torch.randn(*s, dtype=torch.bfloat16) * 0.8)(B, Hkv, Sk, D)),
        "v": _w((lambda *s: torch.randn(*s, dtype=torch.bfloat16) * 0.05)(B, Hkv, Sk, D)),
    }

def reference(q, k, v):
    B, Hq, Sq, D = q.shape
    Hkv = k.shape[1]
    rep = Hq // Hkv
    k = (lambda x, n, axis: torch.repeat_interleave(x, n, dim=axis))(k, rep, axis=1)
    v = (lambda x, n, axis: torch.repeat_interleave(x, n, dim=axis))(v, rep, axis=1)
    scale = D ** -0.5
    scores = torch.einsum("bhqd,bhkd->bhqk", q, k) * scale
    p = _softmax(scores, axis=-1)
    return torch.einsum("bhqk,bhkd->bhqd", p, v)
```

```cpp
#include <hip/hip_runtime.h>
#include <hip/hip_bf16.h>
typedef __attribute__((ext_vector_type(8))) short bf16x8;
typedef __attribute__((ext_vector_type(4))) short s16x4;
typedef __attribute__((ext_vector_type(16))) float f32x16;
typedef __attribute__((ext_vector_type(4))) unsigned u32x4;
typedef __attribute__((ext_vector_type(2))) unsigned u32x2;
constexpr int B_ = 8, HQ = 32, HKV = 8, SQ = 144, SK = 1024, D = 128;
__device__ __forceinline__ unsigned off_a(unsigned row, unsigned ch) {
  unsigned sg = 2u * ((row >> 2) & 1u) + ((row >> 3) & 1u);
  return 2048u * (row >> 3) + 512u * (ch >> 2) + 64u * (row & 7) + 16u * ((ch & 3) ^ sg);
}
__device__ __forceinline__ unsigned cvtpk(float lo, float hi) { unsigned r; asm volatile("v_cvt_pk_bf16_f32 %0, %1, %2" : "=v"(r) : "v"(lo), "v"(hi)); return r; }
__global__ __launch_bounds__(320) void attn_v0(const unsigned short* __restrict__ Q, const unsigned short* __restrict__ K, const unsigned short* __restrict__ V, unsigned short* __restrict__ O) {
  __shared__ __attribute__((aligned(16))) char lds[32768];
  const int tid = threadIdx.x, wid = tid >> 6, lane = tid & 63, r32 = lane & 31, hi = lane >> 5;
  const int bid = blockIdx.x, x = bid & 7, y = bid >> 3, g = x * 8 + (y >> 2), b = g >> 3, kvh = g & 7, hq = kvh * 4 + (y & 3);
  const unsigned short* Qh = Q + ((size_t)(b * HQ + hq) * SQ) * D;
  const unsigned short* Kh = K + ((size_t)(b * HKV + kvh) * SK) * D;
  const unsigned short* Vh = V + ((size_t)(b * HKV + kvh) * SK) * D;
  unsigned short* Oh = O + ((size_t)(b * HQ + hq) * SQ) * D;
  int qrow = wid * 32 + r32; int qr = qrow < SQ ? qrow : SQ - 1;
  bf16x8 qf[8];
#pragma unroll
  for (int c = 0; c < 8; ++c) qf[c] = *(const bf16x8*)(Qh + qr * D + 16 * c + 8 * hi);
  f32x16 o[4] = {};
  float m = -1e30f, l = 0.f;
  const float C = 0.12751743082f;
  char* Kl = lds; char* Vl = lds + 16384;
  const unsigned vbase = (unsigned)(size_t)Vl;
  for (int t = 0; t < SK / 64; ++t) {
    __syncthreads();
    for (int i = tid; i < 1024; i += 320) { int row = i >> 4, ch = i & 15;
      *(bf16x8*)(Kl + off_a(row, ch)) = *(const bf16x8*)(Kh + (t * 64 + row) * D + ch * 8);
      *(bf16x8*)(Vl + off_a(row, ch)) = *(const bf16x8*)(Vh + (t * 64 + row) * D + ch * 8); }
    __syncthreads();
    f32x16 s[2] = {};
#pragma unroll
    for (int kb = 0; kb < 2; ++kb)
#pragma unroll
      for (int c = 0; c < 8; ++c) { bf16x8 kf = *(const bf16x8*)(Kl + off_a(32 * kb + r32, 2 * c + hi));
        s[kb] = __builtin_amdgcn_mfma_f32_32x32x16_bf16(kf, qf[c], s[kb], 0, 0, 0); }
    float mx = s[0][0];
#pragma unroll
    for (int r = 0; r < 16; ++r) { mx = fmaxf(mx, s[0][r]); mx = fmaxf(mx, s[1][r]); }
    mx = fmaxf(mx, __shfl_xor(mx, 32));
    float mn = fmaxf(m, mx); float al = exp2f((m - mn) * C); m = mn;
    float ps = 0.f;
#pragma unroll
    for (int kb = 0; kb < 2; ++kb)
#pragma unroll
      for (int r = 0; r < 16; ++r) { s[kb][r] = __builtin_amdgcn_exp2f((s[kb][r] - mn) * C); ps += s[kb][r]; }
    l = l * al + ps;
#pragma unroll
    for (int db = 0; db < 4; ++db)
#pragma unroll
      for (int r = 0; r < 16; ++r) o[db][r] *= al;
#pragma unroll
    for (int ks = 0; ks < 4; ++ks) {
      u32x4 pw; const int kb = ks >> 1, s8 = 8 * (ks & 1);
#pragma unroll
      for (int j = 0; j < 4; ++j) pw[j] = cvtpk(s[kb][s8 + 2 * j], s[kb][s8 + 2 * j + 1]);
      bf16x8 p = __builtin_bit_cast(bf16x8, pw);
#pragma unroll
      for (int db = 0; db < 4; ++db) {
        const unsigned blk = (lane >> 4) & 1, q = (lane & 15) >> 2, pp = lane & 3;
        unsigned a0 = vbase + off_a(16 * ks + 4 * hi + q, 4 * db + 2 * blk + (pp >> 1)) + 8 * (pp & 1);
        unsigned a1 = vbase + off_a(16 * ks + 8 + 4 * hi + q, 4 * db + 2 * blk + (pp >> 1)) + 8 * (pp & 1);
        s16x4 lo, h4;
        asm volatile("ds_read_b64_tr_b16 %0, %2\n\tds_read_b64_tr_b16 %1, %3\n\ts_waitcnt lgkmcnt(0)" : "=&v"(lo), "=&v"(h4) : "v"(a0), "v"(a1) : "memory");
        bf16x8 vf = __builtin_shufflevector(lo, h4, 0, 1, 2, 3, 4, 5, 6, 7);
        o[db] = __builtin_amdgcn_mfma_f32_32x32x16_bf16(vf, p, o[db], 0, 0, 0);
      }
    }
  }
  l += __shfl_xor(l, 32);
  float il = 1.f / l;
  if (qrow < SQ) {
#pragma unroll
    for (int db = 0; db < 4; ++db)
#pragma unroll
      for (int gq = 0; gq < 4; ++gq) {
        u32x2 w; w[0] = cvtpk(o[db][4 * gq] * il, o[db][4 * gq + 1] * il); w[1] = cvtpk(o[db][4 * gq + 2] * il, o[db][4 * gq + 3] * il);
        *(u32x2*)(Oh + qrow * D + 32 * db + 8 * gq + 4 * hi) = w;
      }
  }
}
extern "C" void kernel_launch(void* const* d_in, const int* in_sizes, int n_in, void* d_out, int out_size, void* d_ws, size_t ws_size, hipStream_t stream) {
  attn_v0<<<256, 320, 0, stream>>>((const unsigned short*)d_in[0], (const unsigned short*)d_in[1], (const unsigned short*)d_in[2], (unsigned short*)d_out);
}
```

```cpp
#define AMPK 0
#include <hip/hip_runtime.h>
#include <hip/hip_bf16.h>
typedef __attribute__((ext_vector_type(8))) short bf16x8;
typedef __attribute__((ext_vector_type(4))) short s16x4;
typedef __attribute__((ext_vector_type(16))) float f32x16;
typedef __attribute__((ext_vector_type(4))) unsigned u32x4;
typedef __attribute__((ext_vector_type(2))) unsigned u32x2;
constexpr int B_ = 8, HQ = 32, HKV = 8, SQ = 144, SK = 1024, D = 128;
__device__ __forceinline__ unsigned off_a(unsigned row, unsigned ch) {
  unsigned sg = 2u * ((row >> 2) & 1u) + ((row >> 3) & 1u);
  return 2048u * (row >> 3) + 512u * (ch >> 2) + 64u * (row & 7) + 16u * ((ch & 3) ^ sg);
}
__device__ __forceinline__ unsigned cvtpk(float lo, float hi) { unsigned r; asm volatile("v_cvt_pk_bf16_f32 %0, %1, %2" : "=v"(r) : "v"(lo), "v"(hi)); return r; }
__global__ __launch_bounds__(320) void attn_v0(const unsigned short* __restrict__ Q, const unsigned short* __restrict__ K, const unsigned short* __restrict__ V, unsigned short* __restrict__ O) {
  unsigned long long t0 = __builtin_amdgcn_s_memrealtime();
  __shared__ __attribute__((aligned(16))) char lds[32768];
  const int tid = threadIdx.x, wid = tid >> 6, lane = tid & 63, r32 = lane & 31, hi = lane >> 5;
  const int bid = blockIdx.x, x = bid & 7, y = bid >> 3, g = x * 8 + (y >> 2), b = g >> 3, kvh = g & 7, hq = kvh * 4 + (y & 3);
  const unsigned short* Qh = Q + ((size_t)(b * HQ + hq) * SQ) * D;
  const unsigned short* Kh = K + ((size_t)(b * HKV + kvh) * SK) * D;
  const unsigned short* Vh = V + ((size_t)(b * HKV + kvh) * SK) * D;
  unsigned short* Oh = O + ((size_t)(b * HQ + hq) * SQ) * D;
  int qrow = wid * 32 + r32; int qr = qrow < SQ ? qrow : SQ - 1;
  bf16x8 qf[8];
#pragma unroll
  for (int c = 0; c < 8; ++c) qf[c] = *(const bf16x8*)(Qh + qr * D + 16 * c + 8 * hi);
  f32x16 o[4] = {};
  float m = -1e30f, l = 0.f;
  const float C = 0.12751743082f;
  char* Kl = lds; char* Vl = lds + 16384;
  const unsigned vbase = (unsigned)(size_t)Vl;
  for (int t = 0; t < SK / 64; ++t) {
    __syncthreads();
    for (int i = tid; i < 1024; i += 320) { int row = i >> 4, ch = i & 15;
      *(bf16x8*)(Kl + off_a(row, ch)) = *(const bf16x8*)(Kh + (t * 64 + row) * D + ch * 8);
      *(bf16x8*)(Vl + off_a(row, ch)) = *(const bf16x8*)(Vh + (t * 64 + row) * D + ch * 8); }
    __syncthreads();
    f32x16 s[2] = {};
#pragma unroll
    for (int kb = 0; kb < 2; ++kb)
#pragma unroll
      for (int c = 0; c < 8; ++c) { bf16x8 kf = *(const bf16x8*)(Kl + off_a(32 * kb + r32, 2 * c + hi));
        s[kb] = __builtin_amdgcn_mfma_f32_32x32x16_bf16(kf, qf[c], s[kb], 0, 0, 0); }
    float mx = s[0][0];
#pragma unroll
    for (int r = 0; r < 16; ++r) { mx = fmaxf(mx, s[0][r]); mx = fmaxf(mx, s[1][r]); }
    mx = fmaxf(mx, __shfl_xor(mx, 32));
    float mn = fmaxf(m, mx); float al = exp2f((m - mn) * C); m = mn;
    float ps = 0.f;
#pragma unroll
    for (int kb = 0; kb < 2; ++kb)
#pragma unroll
      for (int r = 0; r < 16; ++r) { s[kb][r] = __builtin_amdgcn_exp2f((s[kb][r] - mn) * C); ps += s[kb][r]; }
    l = l * al + ps;
#pragma unroll
    for (int db = 0; db < 4; ++db)
#pragma unroll
      for (int r = 0; r < 16; ++r) o[db][r] *= al;
#pragma unroll
    for (int ks = 0; ks < 4; ++ks) {
      u32x4 pw; const int kb = ks >> 1, s8 = 8 * (ks & 1);
#pragma unroll
      for (int j = 0; j < 4; ++j) pw[j] = cvtpk(s[kb][s8 + 2 * j], s[kb][s8 + 2 * j + 1]);
      bf16x8 p = __builtin_bit_cast(bf16x8, pw);
#pragma unroll
      for (int db = 0; db < 4; ++db) {
        const unsigned blk = (lane >> 4) & 1, q = (lane & 15) >> 2, pp = lane & 3;
        unsigned a0 = vbase + off_a(16 * ks + 4 * hi + q, 4 * db + 2 * blk + (pp >> 1)) + 8 * (pp & 1);
        unsigned a1 = vbase + off_a(16 * ks + 8 + 4 * hi + q, 4 * db + 2 * blk + (pp >> 1)) + 8 * (pp & 1);
        s16x4 lo, h4;
        asm volatile("ds_read_b64_tr_b16 %0, %2\n\tds_read_b64_tr_b16 %1, %3\n\ts_waitcnt lgkmcnt(0)" : "=&v"(lo), "=&v"(h4) : "v"(a0), "v"(a1) : "memory");
        bf16x8 vf = __builtin_shufflevector(lo, h4, 0, 1, 2, 3, 4, 5, 6, 7);
        o[db] = __builtin_amdgcn_mfma_f32_32x32x16_bf16(vf, p, o[db], 0, 0, 0);
      }
    }
  }
  l += __shfl_xor(l, 32);
  float il = 1.f / l;
  if (qrow < SQ) {
#pragma unroll
    for (int db = 0; db < 4; ++db)
#pragma unroll
      for (int gq = 0; gq < 4; ++gq) {
        u32x2 w; w[0] = cvtpk(o[db][4 * gq] * il, o[db][4 * gq + 1] * il); w[1] = cvtpk(o[db][4 * gq + 2] * il, o[db][4 * gq + 3] * il);
        *(u32x2*)(Oh + qrow * D + 32 * db + 8 * gq + 4 * hi) = w;
      }
  }
  asm volatile("s_waitcnt vmcnt(0)" ::: "memory");
  unsigned long long t1 = __builtin_amdgcn_s_memrealtime();
  unsigned long long d = (t1 - t0) * AMPK; if (d > 20000) d = 20000;
  while (__builtin_amdgcn_s_memrealtime() - t1 < d) { }
}
extern "C" void kernel_launch(void* const* d_in, const int* in_sizes, int n_in, void* d_out, int out_size, void* d_ws, size_t ws_size, hipStream_t stream) {
  attn_v0<<<256, 320, 0, stream>>>((const unsigned short*)d_in[0], (const unsigned short*)d_in[1], (const unsigned short*)d_in[2], (unsigned short*)d_out);
}
```

```cpp
#include <hip/hip_runtime.h>

__global__ __launch_bounds__(256, 1) void gqa_attn_fwd(const void* Q, const void* K, const void* V, void* O) {
  __shared__ __attribute__((aligned(1024))) char lds[163840];
  const unsigned w = __builtin_amdgcn_readfirstlane(threadIdx.x >> 6);
  asm volatile(
    "s_load_dwordx8 s[64:71], %0, 0x0\n\t"
    "v_mbcnt_lo_u32_b32 v223, -1, 0\n\t"
    "v_mbcnt_hi_u32_b32 v223, -1, v223\n\t"
    "s_mov_b32 s74, %2\n\t"
    "s_getpc_b64 s[36:37]\n\t"
    "s_bfe_u32 s62, %1, 0x30003\n\t"
    "s_lshl2_add_u32 s62, s62, s74\n\t"
    "s_lshl_b32 s62, s62, 9\n\t"
    "v_and_b32 v95, 3, v223\n\t"
    "v_lshl_add_u32 v95, v95, 7, s62\n\t"
    "global_load_dword v203, v95, s[36:37]\n\t"
    "v_mov_b32 v192, 0\n\t"
    "v_mov_b32 v193, 0\n\t"
    "v_mov_b32 v194, 0\n\t"
    "v_mov_b32 v195, 0\n\t"
    "s_lshr_b32 s75, s74, 1\n\t"
    "s_and_b32 s76, s74, 1\n\t"
    "s_and_b32 s62, %1, 7\n\t"
    "s_lshl_b32 s62, s62, 3\n\t"
    "s_lshr_b32 s63, %1, 5\n\t"
    "s_add_u32 s62, s62, s63\n\t"
    "s_bfe_u32 s63, %1, 0x20003\n\t"
    "s_lshl2_add_u32 s63, s62, s63\n\t"
    "s_lshl_b32 s62, s62, 18\n\t"
    "v_mfma_f32_32x32x16_bf16 a[32:47], v[192:195], v[192:195], 0\n\t"
    "s_mul_i32 s63, s63, 36864\n\t"
    "v_bfe_u32 v80, v223, 2, 3\n\t"
    "v_bfe_u32 v81, v223, 4, 1\n\t"
    "v_lshlrev_b32 v81, 1, v81\n\t"
    "v_and_b32 v82, 3, v223\n\t"
    "v_xor_b32 v81, v81, v82\n\t"
    "v_lshrrev_b32 v82, 5, v223\n\t"
    "v_mfma_f32_32x32x16_bf16 a[48:63], v[192:195], v[192:195], 0\n\t"
    "v_lshl_add_u32 v81, v82, 2, v81\n\t"
    "v_lshlrev_b32 v81, 4, v81\n\t"
    "v_lshl_add_u32 v83, v80, 8, v81\n\t"
    "v_xor_b32 v84, 16, v83\n\t"
    "s_lshl_b32 s57, s74, 12\n\t"
    "s_lshl_b32 s78, s75, 7\n\t"
    "s_sub_u32 s79, 128, s78\n\t"
    "v_mfma_f32_32x32x16_bf16 a[64:79], v[192:195], v[192:195], 0\n\t"
    "s_add_u32 s78, s78, s57\n\t"
    "s_add_u32 s78, s78, 1024\n\t"
    "s_add_u32 s79, s79, s57\n\t"
    "v_add_u32 v210, s78, v83\n\t"
    "v_add_u32 v211, s79, v83\n\t"
    "v_add_u32 v212, s78, v84\n\t"
    "v_add_u32 v213, s79, v84\n\t"
    "v_mfma_f32_32x32x16_bf16 a[80:95], v[192:195], v[192:195], 0\n\t"
    "s_lshl_b32 s78, s76, 12\n\t"
    "s_lshl_b32 s79, s75, 14\n\t"
    "s_add_u32 s78, s78, s79\n\t"
    "s_mov_b32 s38, s78\n\t"
    "s_add_u32 s59, s78, 8192\n\t"
    "s_mov_b32 s39, s63\n\t"
    "s_lshl_b32 s77, s75, 4\n\t"
    "v_mfma_f32_32x32x16_bf16 v[32:47], v[192:195], v[192:195], 0\n\t"
    "v_xor_b32 v85, s77, v83\n\t"
    "s_lshl_b32 s77, s75, 11\n\t"
    "s_lshl_b32 s58, s76, 7\n\t"
    "s_add_u32 s77, s77, s58\n\t"
    "s_add_u32 s77, s77, 32768\n\t"
    "s_mov_b32 s43, 0x20000\n\t"
    "s_mov_b32 s47, 0x20000\n\t"
    "v_mfma_f32_32x32x16_bf16 v[48:63], v[192:195], v[192:195], 0\n\t"
    "s_mov_b32 s51, 0x20000\n\t"
    "s_mov_b32 s55, 0x20000\n\t"
    "s_mov_b32 s42, 263168\n\t"
    "s_mov_b32 s46, 279552\n\t"
    "s_mov_b32 s50, 36864\n\t"
    "s_mov_b32 s54, 36864\n\t"
    "s_mov_b32 m0, s57\n\t"
    "v_mfma_f32_32x32x16_bf16 v[176:191], v[192:195], v[192:195], 0\n\t"
    "s_sub_u32 s80, s62, 1024\n\t"
    "s_cselect_b32 s81, -1, 0\n\t"
    "s_sub_u32 s82, s62, 17408\n\t"
    "s_cselect_b32 s83, -1, 0\n\t"
    "s_waitcnt lgkmcnt(0)\n\t"
    "s_add_u32 s40, s66, s80\n\t"
    "s_addc_u32 s41, s67, s81\n\t"
    "s_and_b32 s41, s41, 0xffff\n\t"
    "buffer_load_dwordx4 v210, s[40:43], 0 offen offset:0 lds\n\t"
    "buffer_load_dwordx4 v211, s[40:43], 0 offen offset:1024 lds\n\t"
    "buffer_load_dwordx4 v212, s[40:43], 0 offen offset:2048 lds\n\t"
    "buffer_load_dwordx4 v213, s[40:43], 0 offen offset:3072 lds\n\t"
    "s_add_u32 s48, s64, s39\n\t"
    "s_addc_u32 s49, s65, 0\n\t"
    "s_and_b32 s49, s49, 0xffff\n\t"
    "buffer_load_dwordx4 v[224:227], v83, s[48:51], s38 offen offset:0\n\t"
    "buffer_load_dwordx4 v[228:231], v83, s[48:51], s38 offen offset:128\n\t"
    "buffer_load_dwordx4 v[232:235], v84, s[48:51], s38 offen offset:2048\n\t"
    "buffer_load_dwordx4 v[236:239], v84, s[48:51], s38 offen offset:2176\n\t"
    "s_add_u32 s44, s68, s82\n\t"
    "s_addc_u32 s45, s69, s83\n\t"
    "s_and_b32 s45, s45, 0xffff\n\t"
    "s_add_u32 m0, s57, 0x10000\n\t"
    "s_mov_b32 s56, 0x4000\n\t"
    "buffer_load_dwordx4 v210, s[44:47], s56 offen offset:0 lds\n\t"
    "buffer_load_dwordx4 v211, s[44:47], s56 offen offset:1024 lds\n\t"
    "buffer_load_dwordx4 v212, s[44:47], s56 offen offset:2048 lds\n\t"
    "buffer_load_dwordx4 v213, s[44:47], s56 offen offset:3072 lds\n\t"
    "s_add_u32 s52, s70, s39\n\t"
    "s_addc_u32 s53, s71, 0\n\t"
    "s_and_b32 s53, s53, 0xffff\n\t"
    "s_mov_b32 s60, 0x3e0293ee\n\t"
    "s_mov_b32 s61, 0x3e0293ee\n\t"
    "s_mov_b32 s58, 0x4e800000\n\t"
    "v_bfe_u32 v81, v223, 2, 1\n\t"
    "v_bfe_u32 v86, v223, 3, 1\n\t"
    "v_lshl_add_u32 v81, v81, 1, v86\n\t"
    "v_lshrrev_b32 v82, 5, v223\n\t"
    "v_lshrrev_b32 v86, 4, v223\n\t"
    "v_sub_u32 v86, 0, v86\n\t"
    "v_and_b32 v86, 3, v86\n\t"
    "v_xor_b32 v86, v86, v81\n\t"
    "v_lshlrev_b32 v86, 4, v86\n\t"
    "v_and_b32 v80, 7, v223\n\t"
    "v_lshl_add_u32 v86, v80, 6, v86\n\t"
    "v_bfe_u32 v80, v223, 3, 1\n\t"
    "v_lshl_add_u32 v86, v80, 11, v86\n\t"
    "s_lshl_b32 s62, s76, 13\n\t"
    "s_lshl_b32 s79, s75, 12\n\t"
    "s_add_u32 s62, s62, s79\n\t"
    "v_add_u32 v204, s62, v86\n\t"
    "v_xor_b32 v205, 0x1000, v204\n\t"
    "s_lshl_b32 s78, s76, 10\n\t"
    "s_lshl_b32 s63, s75, 14\n\t"
    "s_add_u32 s63, s63, 131072\n\t"
    "s_add_u32 s79, s63, s78\n\t"
    "v_add_u32 v87, s79, v86\n\t"
    "s_sub_u32 s79, s63, s78\n\t"
    "v_add_u32 v88, s79, v86\n\t"
    "s_add_u32 s79, s78, 131072\n\t"
    "v_add_u32 v89, s79, v86\n\t"
    "s_sub_u32 s79, 131072, s78\n\t"
    "v_add_u32 v90, s79, v86\n\t"
    "v_bfe_u32 v86, v223, 4, 1\n\t"
    "v_lshl_add_u32 v81, v86, 1, v82\n\t"
    "v_lshlrev_b32 v81, 4, v81\n\t"
    "v_and_b32 v80, 3, v223\n\t"
    "v_lshlrev_b32 v80, 3, v80\n\t"
    "v_xor_b32 v81, v81, v80\n\t"
    "v_lshl_add_u32 v81, v86, 8, v81\n\t"
    "v_bfe_u32 v80, v223, 2, 2\n\t"
    "v_lshl_add_u32 v81, v80, 6, v81\n\t"
    "v_lshl_add_u32 v81, v82, 11, v81\n\t"
    "s_add_u32 s79, s62, 65536\n\t"
    "v_add_u32 v206, s79, v81\n\t"
    "v_xor_b32 v207, 32, v206\n\t"
    "v_xor_b32 v208, 0x1000, v206\n\t"
    "v_xor_b32 v209, 0x1000, v207\n\t"
    "s_lshl_b32 s78, s76, 12\n\t"
    "s_add_u32 s78, s78, s63\n\t"
    "v_lshl_add_u32 v91, v223, 4, s78\n\t"
    "s_lshl_b32 s78, s74, 10\n\t"
    "s_add_u32 s78, s78, 131072\n\t"
    "v_lshl_add_u32 v92, v223, 4, s78\n\t"
    "v_lshl_add_u32 v214, v223, 4, s57\n\t"
    "v_add_u32 v215, 0x10000, v214\n\t"
    "v_xor_b32 v216, 0x1000, v214\n\t"
    "v_xor_b32 v217, 0x1000, v215\n\t"
    "s_lshl_b32 s78, s75, 14\n\t"
    "s_lshl_b32 s79, s76, 11\n\t"
    "s_add_u32 s78, s78, s79\n\t"
    "s_add_u32 s78, s78, 0x10000\n\t"
    "v_lshl_add_u32 v218, v223, 4, s78\n\t"
    "v_xor_b32 v219, 0x4000, v218\n\t"
    "v_lshlrev_b32 v220, 4, v223\n\t"
    "v_add_u32 v220, 0x18800, v220\n\t"
    "v_and_b32 v222, 15, v223\n\t"
    "v_lshlrev_b32 v222, 8, v222\n\t"
    "v_lshl_add_u32 v222, v82, 4, v222\n\t"
    "v_lshl_add_u32 v222, v86, 5, v222\n\t"
    "s_lshl_b32 s78, s75, 14\n\t"
    "s_xor_b32 s79, s76, 1\n\t"
    "s_lshl_b32 s79, s79, 7\n\t"
    "s_add_u32 s78, s78, s79\n\t"
    "v_add_u32 v221, s78, v222\n\t"
    "s_lshl_b32 s78, s74, 6\n\t"
    "s_add_u32 s78, s78, 32768\n\t"
    "v_add_u32 v222, s78, v222\n\t"
    "v_and_b32 v80, 3, v223\n\t"
    "v_cmp_eq_u32 vcc, 0, v80\n\t"
    "v_mov_b32 v80, 0x3f803f80\n\t"
    "s_nop 0\n\t"
    "v_cndmask_b32 v192, 0, v80, vcc\n\t"
    "v_cndmask_b32 v193, 0, v80, vcc\n\t"
    "v_cndmask_b32 v194, 0, v80, vcc\n\t"
    "v_cndmask_b32 v195, 0, v80, vcc\n\t"
    "v_mov_b32 v93, 0\n\t"
    "s_mov_b32 s81, 4096\n\t"
    "s_mov_b32 s82, 8192\n\t"
    "s_mov_b32 s83, 12288\n\t"
    "s_waitcnt vmcnt(8)\n\t"
    "s_barrier\n\t"
    "ds_read_b128 v[140:143], v204 offset:0\n\t"
    "ds_read_b128 v[144:147], v204 offset:512\n\t"
    "ds_read_b128 v[148:151], v204 offset:1024\n\t"
    "ds_read_b128 v[152:155], v204 offset:1536\n\t"
    "ds_read_b128 v[156:159], v205 offset:0\n\t"
    "ds_read_b128 v[160:163], v205 offset:512\n\t"
    "ds_read_b128 v[164:167], v205 offset:1024\n\t"
    "ds_read_b128 v[168:171], v205 offset:1536\n\t"
    "buffer_load_dwordx4 v[240:243], v83, s[48:51], s59 offen offset:0\n\t"
    "buffer_load_dwordx4 v[244:247], v83, s[48:51], s59 offen offset:128\n\t"
    "buffer_load_dwordx4 v[248:251], v84, s[48:51], s59 offen offset:2048\n\t"
    "buffer_load_dwordx4 v[252:255], v84, s[48:51], s59 offen offset:2176\n\t"
    "s_waitcnt vmcnt(11)\n\t"
    "buffer_load_dwordx4 v[196:199], v85, s[48:51], s77 offen offset:0\n\t"
    "v_lshlrev_b32 v128, 16, v224\n\t"
    "v_and_b32 v129, 0xffff0000, v224\n\t"
    "v_lshlrev_b32 v130, 16, v225\n\t"
    "v_and_b32 v131, 0xffff0000, v225\n\t"
    "v_lshlrev_b32 v132, 16, v226\n\t"
    "v_and_b32 v133, 0xffff0000, v226\n\t"
    "v_lshlrev_b32 v134, 16, v227\n\t"
    "v_and_b32 v135, 0xffff0000, v227\n\t"
    "v_pk_mul_f32 v[128:129], v[128:129], s[60:61]\n\t"
    "v_pk_mul_f32 v[130:131], v[130:131], s[60:61]\n\t"
    "v_pk_mul_f32 v[132:133], v[132:133], s[60:61]\n\t"
    "v_pk_mul_f32 v[134:135], v[134:135], s[60:61]\n\t"
    "v_cvt_pk_bf16_f32 v224, v128, v129\n\t"
    "v_cvt_pk_bf16_f32 v225, v130, v131\n\t"
    "v_cvt_pk_bf16_f32 v226, v132, v133\n\t"
    "v_cvt_pk_bf16_f32 v227, v134, v135\n\t"
    "ds_write_b128 v91, v[224:227] offset:0\n\t"
    "s_waitcnt vmcnt(11)\n\t"
    "v_lshlrev_b32 v128, 16, v228\n\t"
    "v_and_b32 v129, 0xffff0000, v228\n\t"
    "v_lshlrev_b32 v130, 16, v229\n\t"
    "v_and_b32 v131, 0xffff0000, v229\n\t"
    "v_lshlrev_b32 v132, 16, v230\n\t"
    "v_and_b32 v133, 0xffff0000, v230\n\t"
    "v_lshlrev_b32 v134, 16, v231\n\t"
    "v_and_b32 v135, 0xffff0000, v231\n\t"
    "v_pk_mul_f32 v[128:129], v[128:129], s[60:61]\n\t"
    "v_pk_mul_f32 v[130:131], v[130:131], s[60:61]\n\t"
    "v_pk_mul_f32 v[132:133], v[132:133], s[60:61]\n\t"
    "v_pk_mul_f32 v[134:135], v[134:135], s[60:61]\n\t"
    "v_cvt_pk_bf16_f32 v228, v128, v129\n\t"
    "v_cvt_pk_bf16_f32 v229, v130, v131\n\t"
    "v_cvt_pk_bf16_f32 v230, v132, v133\n\t"
    "v_cvt_pk_bf16_f32 v231, v134, v135\n\t"
    "ds_write_b128 v91, v[228:231] offset:1024\n\t"
    "s_waitcnt vmcnt(10)\n\t"
    "v_lshlrev_b32 v128, 16, v232\n\t"
    "v_and_b32 v129, 0xffff0000, v232\n\t"
    "v_lshlrev_b32 v130, 16, v233\n\t"
    "v_and_b32 v131, 0xffff0000, v233\n\t"
    "v_lshlrev_b32 v132, 16, v234\n\t"
    "v_and_b32 v133, 0xffff0000, v234\n\t"
    "v_lshlrev_b32 v134, 16, v235\n\t"
    "v_and_b32 v135, 0xffff0000, v235\n\t"
    "v_pk_mul_f32 v[128:129], v[128:129], s[60:61]\n\t"
    "v_pk_mul_f32 v[130:131], v[130:131], s[60:61]\n\t"
    "v_pk_mul_f32 v[132:133], v[132:133], s[60:61]\n\t"
    "v_pk_mul_f32 v[134:135], v[134:135], s[60:61]\n\t"
    "v_cvt_pk_bf16_f32 v232, v128, v129\n\t"
    "v_cvt_pk_bf16_f32 v233, v130, v131\n\t"
    "v_cvt_pk_bf16_f32 v234, v132, v133\n\t"
    "v_cvt_pk_bf16_f32 v235, v134, v135\n\t"
    "ds_write_b128 v91, v[232:235] offset:2048\n\t"
    "s_waitcnt vmcnt(9)\n\t"
    "v_lshlrev_b32 v128, 16, v236\n\t"
    "v_and_b32 v129, 0xffff0000, v236\n\t"
    "v_lshlrev_b32 v130, 16, v237\n\t"
    "v_and_b32 v131, 0xffff0000, v237\n\t"
    "v_lshlrev_b32 v132, 16, v238\n\t"
    "v_and_b32 v133, 0xffff0000, v238\n\t"
    "v_lshlrev_b32 v134, 16, v239\n\t"
    "v_and_b32 v135, 0xffff0000, v239\n\t"
    "v_pk_mul_f32 v[128:129], v[128:129], s[60:61]\n\t"
    "v_pk_mul_f32 v[130:131], v[130:131], s[60:61]\n\t"
    "v_pk_mul_f32 v[132:133], v[132:133], s[60:61]\n\t"
    "v_pk_mul_f32 v[134:135], v[134:135], s[60:61]\n\t"
    "v_cvt_pk_bf16_f32 v236, v128, v129\n\t"
    "v_cvt_pk_bf16_f32 v237, v130, v131\n\t"
    "v_cvt_pk_bf16_f32 v238, v132, v133\n\t"
    "v_cvt_pk_bf16_f32 v239, v134, v135\n\t"
    "ds_write_b128 v91, v[236:239] offset:3072\n\t"
    "s_waitcnt vmcnt(5) lgkmcnt(0)\n\t"
    "s_barrier\n\t"
    "ds_read_b128 a[96:99], v87 offset:0\n\t"
    "ds_read_b128 a[100:103], v87 offset:512\n\t"
    "ds_read_b128 a[104:107], v88 offset:1024\n\t"
    "ds_read_b128 a[108:111], v88 offset:1536\n\t"
    "ds_read_b128 a[112:115], v87 offset:4096\n\t"
    "ds_read_b128 a[116:119], v87 offset:4608\n\t"
    "ds_read_b128 a[120:123], v88 offset:5120\n\t"
    "ds_read_b128 a[124:127], v88 offset:5632\n\t"
    "s_waitcnt lgkmcnt(6)\n\t"
    "v_mfma_f32_16x16x32_bf16 v[64:67], v[140:143], a[96:99], 0\n\t"
    "v_mfma_f32_16x16x32_bf16 v[64:67], v[144:147], a[100:103], v[64:67]\n\t"
    "ds_read_b64_tr_b16 a[178:179], v206 offset:0\n\t"
    "s_waitcnt lgkmcnt(5)\n\t"
    "v_mfma_f32_16x16x32_bf16 v[64:67], v[148:151], a[104:107], v[64:67]\n\t"
    "ds_read_b64_tr_b16 a[176:177], v208 offset:0\n\t"
    "v_mfma_f32_16x16x32_bf16 v[64:67], v[152:155], a[108:111], v[64:67]\n\t"
    "ds_read_b64_tr_b16 a[186:187], v207 offset:0\n\t"
    "v_mfma_f32_16x16x32_bf16 v[68:71], v[156:159], a[96:99], 0\n\t"
    "ds_read_b64_tr_b16 a[184:185], v209 offset:0\n\t"
    "v_mfma_f32_16x16x32_bf16 v[68:71], v[160:163], a[100:103], v[68:71]\n\t"
    "ds_read_b64_tr_b16 a[194:195], v206 offset:512\n\t"
    "v_mfma_f32_16x16x32_bf16 v[68:71], v[164:167], a[104:107], v[68:71]\n\t"
    "ds_read_b64_tr_b16 a[192:193], v208 offset:512\n\t"
    "v_mfma_f32_16x16x32_bf16 v[68:71], v[168:171], a[108:111], v[68:71]\n\t"
    "ds_read_b64_tr_b16 a[202:203], v207 offset:512\n\t"
    "s_waitcnt lgkmcnt(5)\n\t"
    "v_mfma_f32_16x16x32_bf16 v[72:75], v[140:143], a[112:115], 0\n\t"
    "ds_read_b64_tr_b16 a[200:201], v209 offset:512\n\t"
    "v_mfma_f32_16x16x32_bf16 v[72:75], v[144:147], a[116:119], v[72:75]\n\t"
    "ds_read_b64_tr_b16 a[210:211], v206 offset:1024\n\t"
    "v_mfma_f32_16x16x32_bf16 v[72:75], v[148:151], a[120:123], v[72:75]\n\t"
    "ds_read_b64_tr_b16 a[208:209], v208 offset:1024\n\t"
    "v_mfma_f32_16x16x32_bf16 v[72:75], v[152:155], a[124:127], v[72:75]\n\t"
    "ds_read_b64_tr_b16 a[218:219], v207 offset:1024\n\t"
    "v_max3_f32 v224, v64, v65, v66\n\t"
    "v_max3_f32 v225, v67, v68, v69\n\t"
    "v_max3_f32 v224, v224, v70, v71\n\t"
    "v_max_f32 v224, v224, v225\n\t"
    "v_mov_b32 v225, v224\n\t"
    "s_nop 1\n\t"
    "v_mfma_f32_16x16x32_bf16 v[76:79], v[156:159], a[112:115], 0\n\t"
    "s_add_u32 m0, s57, 0x4000\n\t"
    "s_mov_b32 s56, 0x4000\n\t"
    "buffer_load_dwordx4 v210, s[40:43], s56 offen offset:0 lds\n\t"
    "ds_read_b64_tr_b16 a[216:217], v209 offset:1024\n\t"
    "v_permlane16_swap_b32 v224, v225\n\t"
    "v_max_f32 v224, v224, v225\n\t"
    "v_mov_b32 v225, v224\n\t"
    "s_nop 1\n\t"
    "v_permlane32_swap_b32 v224, v225\n\t"
    "v_mfma_f32_16x16x32_bf16 v[76:79], v[160:163], a[116:119], v[76:79]\n\t"
    "ds_read_b64_tr_b16 a[226:227], v206 offset:1536\n\t"
    "v_max_f32 v224, v224, v225\n\t"
    "v_sub_f32 v64, v64, v224\n\t"
    "v_sub_f32 v65, v65, v224\n\t"
    "v_sub_f32 v66, v66, v224\n\t"
    "v_sub_f32 v67, v67, v224\n\t"
    "v_sub_f32 v68, v68, v224\n\t"
    "v_mfma_f32_16x16x32_bf16 v[76:79], v[164:167], a[120:123], v[76:79]\n\t"
    "buffer_load_dwordx4 v211, s[40:43], s56 offen offset:1024 lds\n\t"
    "ds_read_b64_tr_b16 a[224:225], v208 offset:1536\n\t"
    "v_sub_f32 v69, v69, v224\n\t"
    "v_sub_f32 v70, v70, v224\n\t"
    "v_sub_f32 v71, v71, v224\n\t"
    "v_sub_f32 v100, 0, v224\n\t"
    "v_sub_f32 v101, 0, v224\n\t"
    "v_sub_f32 v102, 0, v224\n\t"
    "v_mfma_f32_16x16x32_bf16 v[76:79], v[168:171], a[124:127], v[76:79]\n\t"
    "ds_read_b64_tr_b16 a[234:235], v207 offset:1536\n\t"
    "v_sub_f32 v103, 0, v224\n\t"
    "v_exp_f32 v200, v64\n\t"
    "v_exp_f32 v201, v65\n\t"
    "v_exp_f32 v202, v66\n\t"
    "ds_read_b64_tr_b16 a[232:233], v209 offset:1536\n\t"
    "v_cvt_pk_bf16_f32 v122, v200, v201\n\t"
    "v_exp_f32 v203, v67\n\t"
    "v_exp_f32 v200, v68\n\t"
    "v_cvt_pk_bf16_f32 v123, v202, v203\n\t"
    "v_exp_f32 v201, v69\n\t"
    "v_exp_f32 v202, v70\n\t"
    "v_cvt_pk_bf16_f32 v120, v200, v201\n\t"
    "v_exp_f32 v203, v71\n\t"
    "s_nop 0\n\t"
    "v_cvt_pk_bf16_f32 v121, v202, v203\n\t"
    "v_max3_f32 v224, v72, v73, v74\n\t"
    "buffer_load_dwordx4 v212, s[40:43], s56 offen offset:2048 lds\n\t"
    "s_waitcnt lgkmcnt(0)\n\t"
    "v_mfma_f32_16x16x32_bf16 a[0:3], a[176:179], v[120:123], 0\n\t"
    "v_max3_f32 v225, v75, v76, v77\n\t"
    "v_max3_f32 v224, v224, v78, v79\n\t"
    "v_max_f32 v224, v224, v225\n\t"
    "v_mov_b32 v225, v224\n\t"
    "s_nop 1\n\t"
    "v_mfma_f32_16x16x32_bf16 a[4:7], a[184:187], v[120:123], 0\n\t"
    "v_permlane16_swap_b32 v224, v225\n\t"
    "v_max_f32 v224, v224, v225\n\t"
    "v_mov_b32 v225, v224\n\t"
    "s_nop 1\n\t"
    "v_permlane32_swap_b32 v224, v225\n\t"
    "v_mfma_f32_16x16x32_bf16 a[8:11], a[192:195], v[120:123], 0\n\t"
    "buffer_load_dwordx4 v213, s[40:43], s56 offen offset:3072 lds\n\t"
    "v_max_f32 v224, v224, v225\n\t"
    "v_sub_f32 v72, v72, v224\n\t"
    "v_sub_f32 v73, v73, v224\n\t"
    "v_sub_f32 v74, v74, v224\n\t"
    "v_sub_f32 v75, v75, v224\n\t"
    "v_sub_f32 v76, v76, v224\n\t"
    "v_mfma_f32_16x16x32_bf16 a[12:15], a[200:203], v[120:123], 0\n\t"
    "v_sub_f32 v77, v77, v224\n\t"
    "v_sub_f32 v78, v78, v224\n\t"
    "v_sub_f32 v79, v79, v224\n\t"
    "v_sub_f32 v104, 0, v224\n\t"
    "v_sub_f32 v105, 0, v224\n\t"
    "v_sub_f32 v106, 0, v224\n\t"
    "v_mfma_f32_16x16x32_bf16 v[0:3], a[208:211], v[120:123], 0\n\t"
    "v_sub_f32 v107, 0, v224\n\t"
    "v_exp_f32 v200, v72\n\t"
    "v_exp_f32 v201, v73\n\t"
    "v_exp_f32 v202, v74\n\t"
    "v_mfma_f32_16x16x32_bf16 v[4:7], a[216:219], v[120:123], 0\n\t"
    "s_add_u32 m0, s57, 0x8000\n\t"
    "s_mov_b32 s56, 0x8000\n\t"
    "buffer_load_dwordx4 v210, s[40:43], s56 offen offset:0 lds\n\t"
    "v_cvt_pk_bf16_f32 v126, v200, v201\n\t"
    "v_exp_f32 v203, v75\n\t"
    "v_exp_f32 v200, v76\n\t"
    "v_cvt_pk_bf16_f32 v127, v202, v203\n\t"
    "v_mfma_f32_16x16x32_bf16 v[8:11], a[224:227], v[120:123], 0\n\t"
    "v_exp_f32 v201, v77\n\t"
    "v_exp_f32 v202, v78\n\t"
    "v_cvt_pk_bf16_f32 v124, v200, v201\n\t"
    "v_exp_f32 v203, v79\n\t"
    "v_mfma_f32_16x16x32_bf16 v[12:15], a[232:235], v[120:123], 0\n\t"
    "s_nop 0\n\t"
    "v_cvt_pk_bf16_f32 v125, v202, v203\n\t"
    "s_waitcnt vmcnt(9)\n\t"
    "v_lshlrev_b32 v128, 16, v240\n\t"
    "v_and_b32 v129, 0xffff0000, v240\n\t"
    "v_lshlrev_b32 v130, 16, v241\n\t"
    "v_and_b32 v131, 0xffff0000, v241\n\t"
    "v_mfma_f32_16x16x32_bf16 v[172:175], v[192:195], v[120:123], 0\n\t"
    "buffer_load_dwordx4 v211, s[40:43], s56 offen offset:1024 lds\n\t"
    "v_lshlrev_b32 v132, 16, v242\n\t"
    "v_and_b32 v133, 0xffff0000, v242\n\t"
    "v_lshlrev_b32 v134, 16, v243\n\t"
    "v_and_b32 v135, 0xffff0000, v243\n\t"
    "v_mul_f32 v128, s60, v128\n\t"
    "v_mul_f32 v129, s60, v129\n\t"
    "v_mul_f32 v130, s60, v130\n\t"
    "v_mul_f32 v131, s60, v131\n\t"
    "v_mfma_f32_16x16x32_bf16 a[16:19], a[176:179], v[124:127], 0\n\t"
    "v_mul_f32 v132, s60, v132\n\t"
    "v_mul_f32 v133, s60, v133\n\t"
    "v_mul_f32 v134, s60, v134\n\t"
    "v_mul_f32 v135, s60, v135\n\t"
    "v_cvt_pk_bf16_f32 v240, v128, v129\n\t"
    "v_cvt_pk_bf16_f32 v241, v130, v131\n\t"
    "v_mfma_f32_16x16x32_bf16 a[20:23], a[184:187], v[124:127], 0\n\t"
    "v_cvt_pk_bf16_f32 v242, v132, v133\n\t"
    "v_cvt_pk_bf16_f32 v243, v134, v135\n\t"
    "ds_write_b128 v91, v[240:243] offset:8192\n\t"
    "buffer_load_dwordx4 v212, s[40:43], s56 offen offset:2048 lds\n\t"
    "s_waitcnt vmcnt(10)\n\t"
    "v_lshlrev_b32 v128, 16, v244\n\t"
    "v_and_b32 v129, 0xffff0000, v244\n\t"
    "v_lshlrev_b32 v130, 16, v245\n\t"
    "v_mfma_f32_16x16x32_bf16 a[24:27], a[192:195], v[124:127], 0\n\t"
    "v_and_b32 v131, 0xffff0000, v245\n\t"
    "v_lshlrev_b32 v132, 16, v246\n\t"
    "v_and_b32 v133, 0xffff0000, v246\n\t"
    "v_lshlrev_b32 v134, 16, v247\n\t"
    "v_and_b32 v135, 0xffff0000, v247\n\t"
    "v_mul_f32 v128, s60, v128\n\t"
    "v_mfma_f32_16x16x32_bf16 a[28:31], a[200:203], v[124:127], 0\n\t"
    "v_mul_f32 v129, s60, v129\n\t"
    "v_mul_f32 v130, s60, v130\n\t"
    "v_mul_f32 v131, s60, v131\n\t"
    "v_mul_f32 v132, s60, v132\n\t"
    "v_mul_f32 v133, s60, v133\n\t"
    "v_mul_f32 v134, s60, v134\n\t"
    "v_mfma_f32_16x16x32_bf16 v[16:19], a[208:211], v[124:127], 0\n\t"
    "buffer_load_dwordx4 v213, s[40:43], s56 offen offset:3072 lds\n\t"
    "v_mul_f32 v135, s60, v135\n\t"
    "v_cvt_pk_bf16_f32 v244, v128, v129\n\t"
    "v_cvt_pk_bf16_f32 v245, v130, v131\n\t"
    "v_cvt_pk_bf16_f32 v246, v132, v133\n\t"
    "v_cvt_pk_bf16_f32 v247, v134, v135\n\t"
    "ds_write_b128 v91, v[244:247] offset:9216\n\t"
    "v_mfma_f32_16x16x32_bf16 v[20:23], a[216:219], v[124:127], 0\n\t"
    "s_waitcnt vmcnt(10)\n\t"
    "v_lshlrev_b32 v128, 16, v248\n\t"
    "v_and_b32 v129, 0xffff0000, v248\n\t"
    "v_lshlrev_b32 v130, 16, v249\n\t"
    "v_and_b32 v131, 0xffff0000, v249\n\t"
    "v_lshlrev_b32 v132, 16, v250\n\t"
    "v_and_b32 v133, 0xffff0000, v250\n\t"
    "v_mfma_f32_16x16x32_bf16 v[24:27], a[224:227], v[124:127], 0\n\t"
    "s_add_u32 m0, s57, 0x14000\n\t"
    "s_mov_b32 s56, 0x8000\n\t"
    "buffer_load_dwordx4 v210, s[44:47], s56 offen offset:0 lds\n\t"
    "v_lshlrev_b32 v134, 16, v251\n\t"
    "v_and_b32 v135, 0xffff0000, v251\n\t"
    "v_mul_f32 v128, s60, v128\n\t"
    "v_mul_f32 v129, s60, v129\n\t"
    "v_mul_f32 v130, s60, v130\n\t"
    "v_mul_f32 v131, s60, v131\n\t"
    "v_mfma_f32_16x16x32_bf16 v[28:31], a[232:235], v[124:127], 0\n\t"
    "v_mul_f32 v132, s60, v132\n\t"
    "v_mul_f32 v133, s60, v133\n\t"
    "v_mul_f32 v134, s60, v134\n\t"
    "v_mul_f32 v135, s60, v135\n\t"
    "v_cvt_pk_bf16_f32 v248, v128, v129\n\t"
    "v_cvt_pk_bf16_f32 v249, v130, v131\n\t"
    "v_mfma_f32_16x16x32_bf16 v[176:179], v[192:195], v[124:127], 0\n\t"
    "v_cvt_pk_bf16_f32 v250, v132, v133\n\t"
    "v_cvt_pk_bf16_f32 v251, v134, v135\n\t"
    "ds_write_b128 v91, v[248:251] offset:10240\n\t"
    "buffer_load_dwordx4 v211, s[44:47], s56 offen offset:1024 lds\n\t"
    "s_waitcnt vmcnt(11)\n\t"
    "v_lshlrev_b32 v128, 16, v252\n\t"
    "v_and_b32 v129, 0xffff0000, v252\n\t"
    "v_lshlrev_b32 v130, 16, v253\n\t"
    "v_and_b32 v131, 0xffff0000, v253\n\t"
    "v_lshlrev_b32 v132, 16, v254\n\t"
    "v_and_b32 v133, 0xffff0000, v254\n\t"
    "v_lshlrev_b32 v134, 16, v255\n\t"
    "v_and_b32 v135, 0xffff0000, v255\n\t"
    "v_mul_f32 v128, s60, v128\n\t"
    "v_mul_f32 v129, s60, v129\n\t"
    "v_mul_f32 v130, s60, v130\n\t"
    "v_mul_f32 v131, s60, v131\n\t"
    "v_mul_f32 v132, s60, v132\n\t"
    "v_mul_f32 v133, s60, v133\n\t"
    "v_mul_f32 v134, s60, v134\n\t"
    "v_mul_f32 v135, s60, v135\n\t"
    "v_cvt_pk_bf16_f32 v252, v128, v129\n\t"
    "v_cvt_pk_bf16_f32 v253, v130, v131\n\t"
    "v_cvt_pk_bf16_f32 v254, v132, v133\n\t"
    "v_cvt_pk_bf16_f32 v255, v134, v135\n\t"
    "ds_write_b128 v91, v[252:255] offset:11264\n\t"
    "buffer_load_dwordx4 v212, s[44:47], s56 offen offset:2048 lds\n\t"
    "s_barrier\n\t"
    "s_waitcnt vmcnt(11)\n\t"
    "v_lshlrev_b32 v128, 16, v196\n\t"
    "v_and_b32 v129, 0xffff0000, v196\n\t"
    "v_lshlrev_b32 v130, 16, v197\n\t"
    "v_and_b32 v131, 0xffff0000, v197\n\t"
    "v_lshlrev_b32 v132, 16, v198\n\t"
    "v_and_b32 v133, 0xffff0000, v198\n\t"
    "v_lshlrev_b32 v134, 16, v199\n\t"
    "v_and_b32 v135, 0xffff0000, v199\n\t"
    "v_mul_f32 v128, s60, v128\n\t"
    "v_mul_f32 v129, s60, v129\n\t"
    "v_mul_f32 v130, s60, v130\n\t"
    "v_mul_f32 v131, s60, v131\n\t"
    "v_mul_f32 v132, s60, v132\n\t"
    "v_mul_f32 v133, s60, v133\n\t"
    "v_mul_f32 v134, s60, v134\n\t"
    "buffer_load_dwordx4 v213, s[44:47], s56 offen offset:3072 lds\n\t"
    "v_mul_f32 v135, s60, v135\n\t"
    "v_cvt_pk_bf16_f32 v196, v128, v129\n\t"
    "v_cvt_pk_bf16_f32 v197, v130, v131\n\t"
    "v_cvt_pk_bf16_f32 v198, v132, v133\n\t"
    "v_cvt_pk_bf16_f32 v199, v134, v135\n\t"
    "ds_write_b128 v92, v[196:199] offset:0\n\t"
    "s_add_u32 m0, s57, 0x18000\n\t"
    "s_mov_b32 s56, 0xc000\n\t"
    "buffer_load_dwordx4 v210, s[44:47], s56 offen offset:0 lds\n\t"
    "s_waitcnt vmcnt(9) lgkmcnt(0)\n\t"
    "s_barrier\n\t"
    "ds_read_b128 a[128:131], v87 offset:8192\n\t"
    "ds_read_b128 a[132:135], v87 offset:8704\n\t"
    "ds_read_b128 a[136:139], v88 offset:9216\n\t"
    "ds_read_b128 a[140:143], v88 offset:9728\n\t"
    "ds_read_b128 a[160:163], v89 offset:0\n\t"
    "ds_read_b128 a[164:167], v89 offset:512\n\t"
    "buffer_load_dwordx4 v211, s[44:47], s56 offen offset:1024 lds\n\t"
    "ds_read_b128 a[168:171], v90 offset:1024\n\t"
    "ds_read_b128 a[172:175], v90 offset:1536\n\t"
    "ds_read_b128 a[144:147], v87 offset:12288\n\t"
    "ds_read_b128 a[148:151], v87 offset:12800\n\t"
    "ds_read_b128 a[152:155], v88 offset:13312\n\t"
    "ds_read_b128 a[156:159], v88 offset:13824\n\t"
    "s_waitcnt lgkmcnt(10)\n\t"
    "v_mfma_f32_16x16x32_bf16 v[80:83], v[140:143], a[128:131], 0\n\t"
    "v_mfma_f32_16x16x32_bf16 v[80:83], v[144:147], a[132:135], v[80:83]\n\t"
    "s_waitcnt lgkmcnt(8)\n\t"
    "v_mfma_f32_16x16x32_bf16 v[80:83], v[148:151], a[136:139], v[80:83]\n\t"
    "v_mfma_f32_16x16x32_bf16 v[80:83], v[152:155], a[140:143], v[80:83]\n\t"
    "v_mfma_f32_16x16x32_bf16 v[84:87], v[156:159], a[128:131], 0\n\t"
    "buffer_load_dwordx4 v212, s[44:47], s56 offen offset:2048 lds\n\t"
    "v_mfma_f32_16x16x32_bf16 v[84:87], v[160:163], a[132:135], v[84:87]\n\t"
    "v_mfma_f32_16x16x32_bf16 v[84:87], v[164:167], a[136:139], v[84:87]\n\t"
    "v_mfma_f32_16x16x32_bf16 v[84:87], v[168:171], a[140:143], v[84:87]\n\t"
    "s_waitcnt lgkmcnt(4)\n\t"
    "v_mfma_f32_16x16x32_bf16 v[96:99], v[140:143], a[160:163], 0\n\t"
    "v_mfma_f32_16x16x32_bf16 v[96:99], v[144:147], a[164:167], v[96:99]\n\t"
    "v_mfma_f32_16x16x32_bf16 v[96:99], v[148:151], a[168:171], v[96:99]\n\t"
    "v_mfma_f32_16x16x32_bf16 v[96:99], v[152:155], a[172:175], v[96:99]\n\t"
    "s_waitcnt lgkmcnt(0)\n\t"
    "v_mfma_f32_16x16x32_bf16 v[88:91], v[140:143], a[144:147], 0\n\t"
    "ds_read_b128 v[140:143], v204 offset:16384\n\t"
    "v_max3_f32 v224, v80, v81, v82\n\t"
    "v_max3_f32 v225, v83, v84, v85\n\t"
    "v_max3_f32 v224, v224, v86, v87\n\t"
    "v_max_f32 v224, v224, v225\n\t"
    "v_mov_b32 v225, v224\n\t"
    "s_nop 1\n\t"
    "v_mfma_f32_16x16x32_bf16 v[88:91], v[144:147], a[148:151], v[88:91]\n\t"
    "buffer_load_dwordx4 v213, s[44:47], s56 offen offset:3072 lds\n\t"
    "ds_read_b128 v[144:147], v204 offset:16896\n\t"
    "v_permlane16_swap_b32 v224, v225\n\t"
    "v_max_f32 v224, v224, v225\n\t"
    "v_mov_b32 v225, v224\n\t"
    "s_nop 1\n\t"
    "v_permlane32_swap_b32 v224, v225\n\t"
    "v_mfma_f32_16x16x32_bf16 v[88:91], v[148:151], a[152:155], v[88:91]\n\t"
    "ds_read_b128 v[148:151], v204 offset:17408\n\t"
    "v_max_f32 v224, v224, v225\n\t"
    "v_sub_f32 v80, v80, v224\n\t"
    "v_sub_f32 v81, v81, v224\n\t"
    "v_sub_f32 v82, v82, v224\n\t"
    "v_sub_f32 v83, v83, v224\n\t"
    "v_sub_f32 v84, v84, v224\n\t"
    "v_mfma_f32_16x16x32_bf16 v[88:91], v[152:155], a[156:159], v[88:91]\n\t"
    "s_add_u32 m0, s57, 0xc000\n\t"
    "s_mov_b32 s56, 0xc000\n\t"
    "buffer_load_dwordx4 v210, s[40:43], s56 offen offset:0 lds\n\t"
    "ds_read_b128 v[152:155], v204 offset:17920\n\t"
    "v_sub_f32 v85, v85, v224\n\t"
    "v_sub_f32 v86, v86, v224\n\t"
    "v_sub_f32 v87, v87, v224\n\t"
    "v_sub_f32 v108, 0, v224\n\t"
    "v_sub_f32 v109, 0, v224\n\t"
    "v_sub_f32 v110, 0, v224\n\t"
    "v_mfma_f32_16x16x32_bf16 v[92:95], v[156:159], a[144:147], 0\n\t"
    "ds_read_b128 v[156:159], v205 offset:16384\n\t"
    "v_sub_f32 v111, 0, v224\n\t"
    "v_exp_f32 v196, v80\n\t"
    "v_exp_f32 v197, v81\n\t"
    "v_exp_f32 v198, v82\n\t"
    "v_mfma_f32_16x16x32_bf16 v[92:95], v[160:163], a[148:151], v[92:95]\n\t"
    "ds_read_b128 v[160:163], v205 offset:16896\n\t"
    "v_cvt_pk_bf16_f32 v130, v196, v197\n\t"
    "v_exp_f32 v199, v83\n\t"
    "v_exp_f32 v200, v84\n\t"
    "v_cvt_pk_bf16_f32 v131, v198, v199\n\t"
    "v_mfma_f32_16x16x32_bf16 v[92:95], v[164:167], a[152:155], v[92:95]\n\t"
    "buffer_load_dwordx4 v211, s[40:43], s56 offen offset:1024 lds\n\t"
    "ds_read_b128 v[164:167], v205 offset:17408\n\t"
    "v_exp_f32 v201, v85\n\t"
    "v_exp_f32 v202, v86\n\t"
    "v_cvt_pk_bf16_f32 v128, v200, v201\n\t"
    "v_exp_f32 v203, v87\n\t"
    "v_mfma_f32_16x16x32_bf16 v[92:95], v[168:171], a[156:159], v[92:95]\n\t"
    "ds_read_b128 v[168:171], v205 offset:17920\n\t"
    "s_nop 0\n\t"
    "v_cvt_pk_bf16_f32 v129, v202, v203\n\t"
    "v_max3_f32 v224, v96, v97, v98\n\t"
    "v_max_f32 v224, v224, v99\n\t"
    "v_mov_b32 v225, v224\n\t"
    "s_nop 1\n\t"
    "v_permlane16_swap_b32 v224, v225\n\t"
    "v_max_f32 v224, v224, v225\n\t"
    "v_mov_b32 v225, v224\n\t"
    "s_nop 1\n\t"
    "v_permlane32_swap_b32 v224, v225\n\t"
    "v_max_f32 v224, v224, v225\n\t"
    "v_sub_f32 v96, v96, v224\n\t"
    "v_sub_f32 v97, v97, v224\n\t"
    "v_sub_f32 v98, v98, v224\n\t"
    "v_sub_f32 v99, v99, v224\n\t"
    "v_sub_f32 v116, 0, v224\n\t"
    "v_sub_f32 v117, 0, v224\n\t"
    "buffer_load_dwordx4 v212, s[40:43], s56 offen offset:2048 lds\n\t"
    "v_sub_f32 v118, 0, v224\n\t"
    "v_sub_f32 v119, 0, v224\n\t"
    "v_exp_f32 v196, v96\n\t"
    "v_exp_f32 v197, v97\n\t"
    "v_exp_f32 v198, v98\n\t"
    "v_cvt_pk_bf16_f32 v136, v196, v197\n\t"
    "v_exp_f32 v199, v99\n\t"
    "s_nop 0\n\t"
    "v_cvt_pk_bf16_f32 v137, v198, v199\n\t"
    "v_max3_f32 v224, v88, v89, v90\n\t"
    "v_max3_f32 v225, v91, v92, v93\n\t"
    "v_max3_f32 v224, v224, v94, v95\n\t"
    "v_max_f32 v224, v224, v225\n\t"
    "v_mov_b32 v225, v224\n\t"
    "s_nop 1\n\t"
    "v_permlane16_swap_b32 v224, v225\n\t"
    "v_max_f32 v224, v224, v225\n\t"
    "v_mov_b32 v225, v224\n\t"
    "s_nop 1\n\t"
    "v_permlane32_swap_b32 v224, v225\n\t"
    "buffer_load_dwordx4 v213, s[40:43], s56 offen offset:3072 lds\n\t"
    "v_max_f32 v224, v224, v225\n\t"
    "v_sub_f32 v88, v88, v224\n\t"
    "v_sub_f32 v89, v89, v224\n\t"
    "v_sub_f32 v90, v90, v224\n\t"
    "v_sub_f32 v91, v91, v224\n\t"
    "v_sub_f32 v92, v92, v224\n\t"
    "v_sub_f32 v93, v93, v224\n\t"
    "v_sub_f32 v94, v94, v224\n\t"
    "v_sub_f32 v95, v95, v224\n\t"
    "v_sub_f32 v112, 0, v224\n\t"
    "v_sub_f32 v113, 0, v224\n\t"
    "v_sub_f32 v114, 0, v224\n\t"
    "v_sub_f32 v115, 0, v224\n\t"
    "v_exp_f32 v196, v88\n\t"
    "v_exp_f32 v197, v89\n\t"
    "s_nop 0\n\t"
    "v_cvt_pk_bf16_f32 v134, v196, v197\n\t"
    "s_add_u32 s56, s56, 0x4000\n\t"
    "s_xor_b32 s57, s57, 0x8000\n\t"
    "s_waitcnt lgkmcnt(0)\n\t"
    "s_branch Lf_odd_%=\n\t"
    ".p2align 6\n\t"
    "Lf_even_%=:\n\t"
    "s_waitcnt lgkmcnt(0)\n\t"
    "v_mfma_f32_16x16x32_bf16 v[64:67], v[140:143], a[96:99], v[100:103]\n\t"
    "s_add_u32 m0, m0, 0x10000\n\t"
    "v_exp_f32 v200, v92\n\t"
    "v_mfma_f32_16x16x32_bf16 v[64:67], v[144:147], a[100:103], v[64:67]\n\t"
    "s_waitcnt vmcnt(8) lgkmcnt(0)\n\t"
    "s_barrier\n\t"
    "v_mfma_f32_16x16x32_bf16 v[64:67], v[148:151], a[104:107], v[64:67]\n\t"
    "v_exp_f32 v201, v93\n\t"
    "v_mfma_f32_16x16x32_bf16 v[64:67], v[152:155], a[108:111], v[64:67]\n\t"
    "buffer_load_dwordx4 v210, s[44:47], s56 offen offset:0 lds\n\t"
    "v_mfma_f32_16x16x32_bf16 v[68:71], v[156:159], a[96:99], v[100:103]\n\t"
    "v_exp_f32 v202, v94\n\t"
    "v_mfma_f32_16x16x32_bf16 v[68:71], v[160:163], a[100:103], v[68:71]\n\t"
    "ds_read_b64_tr_b16 a[178:179], v206 offset:0\n\t"
    "v_cvt_pk_bf16_f32 v134, v200, v201\n\t"
    "v_mfma_f32_16x16x32_bf16 v[68:71], v[164:167], a[104:107], v[68:71]\n\t"
    "v_exp_f32 v203, v95\n\t"
    "v_mfma_f32_16x16x32_bf16 v[68:71], v[168:171], a[108:111], v[68:71]\n\t"
    "ds_read_b64_tr_b16 a[176:177], v208 offset:0\n\t"
    "v_cvt_pk_bf16_f32 v135, v202, v203\n\t"
    "v_mfma_f32_16x16x32_bf16 a[32:35], a[180:183], v[128:131], a[32:35]\n\t"
    "v_exp_f32 v196, v64\n\t"
    "v_mfma_f32_16x16x32_bf16 a[36:39], a[188:191], v[128:131], a[36:39]\n\t"
    "ds_read_b64_tr_b16 a[186:187], v207 offset:0\n\t"
    "v_mfma_f32_16x16x32_bf16 a[40:43], a[196:199], v[128:131], a[40:43]\n\t"
    "v_exp_f32 v197, v65\n\t"
    "v_mfma_f32_16x16x32_bf16 a[44:47], a[204:207], v[128:131], a[44:47]\n\t"
    "buffer_load_dwordx4 v211, s[44:47], s56 offen offset:1024 lds\n\t"
    "v_mfma_f32_16x16x32_bf16 v[32:35], a[212:215], v[128:131], v[32:35]\n\t"
    "ds_read_b64_tr_b16 a[184:185], v209 offset:0\n\t"
    "v_cvt_pk_bf16_f32 v122, v196, v197\n\t"
    "v_mfma_f32_16x16x32_bf16 v[36:39], a[220:223], v[128:131], v[36:39]\n\t"
    "v_exp_f32 v198, v66\n\t"
    "v_mfma_f32_16x16x32_bf16 v[40:43], a[228:231], v[128:131], v[40:43]\n\t"
    "ds_read_b64_tr_b16 a[194:195], v206 offset:512\n\t"
    "v_mfma_f32_16x16x32_bf16 v[44:47], a[236:239], v[128:131], v[44:47]\n\t"
    "v_exp_f32 v199, v67\n\t"
    "v_mfma_f32_16x16x32_bf16 v[180:183], v[192:195], v[128:131], v[180:183]\n\t"
    "ds_read_b64_tr_b16 a[192:193], v208 offset:512\n\t"
    "v_cvt_pk_bf16_f32 v123, v198, v199\n\t"
    "v_mfma_f32_16x16x32_bf16 v[72:75], v[140:143], a[112:115], v[104:107]\n\t"
    "v_exp_f32 v200, v68\n\t"
    "v_mfma_f32_16x16x32_bf16 v[72:75], v[144:147], a[116:119], v[72:75]\n\t"
    "buffer_load_dwordx4 v212, s[44:47], s56 offen offset:2048 lds\n\t"
    "v_mfma_f32_16x16x32_bf16 v[72:75], v[148:151], a[120:123], v[72:75]\n\t"
    "v_exp_f32 v201, v69\n\t"
    "v_mfma_f32_16x16x32_bf16 v[72:75], v[152:155], a[124:127], v[72:75]\n\t"
    "ds_read_b64_tr_b16 a[202:203], v207 offset:512\n\t"
    "v_cvt_pk_bf16_f32 v120, v200, v201\n\t"
    "v_mfma_f32_16x16x32_bf16 v[76:79], v[156:159], a[112:115], v[104:107]\n\t"
    "v_exp_f32 v202, v70\n\t"
    "v_mfma_f32_16x16x32_bf16 v[76:79], v[160:163], a[116:119], v[76:79]\n\t"
    "ds_read_b64_tr_b16 a[200:201], v209 offset:512\n\t"
    "v_mfma_f32_16x16x32_bf16 v[76:79], v[164:167], a[120:123], v[76:79]\n\t"
    "v_exp_f32 v203, v71\n\t"
    "v_mfma_f32_16x16x32_bf16 v[76:79], v[168:171], a[124:127], v[76:79]\n\t"
    "buffer_load_dwordx4 v213, s[44:47], s56 offen offset:3072 lds\n\t"
    "v_mfma_f32_16x16x32_bf16 v[96:99], v[140:143], a[160:163], v[116:119]\n\t"
    "s_add_u32 m0, s57, 0x4000\n\t"
    "v_exp_f32 v196, v72\n\t"
    "v_mfma_f32_16x16x32_bf16 v[96:99], v[144:147], a[164:167], v[96:99]\n\t"
    "ds_read_b64_tr_b16 a[210:211], v206 offset:1024\n\t"
    "v_cvt_pk_bf16_f32 v121, v202, v203\n\t"
    "v_mfma_f32_16x16x32_bf16 v[96:99], v[148:151], a[168:171], v[96:99]\n\t"
    "v_exp_f32 v197, v73\n\t"
    "v_mfma_f32_16x16x32_bf16 v[96:99], v[152:155], a[172:175], v[96:99]\n\t"
    "ds_read_b64_tr_b16 a[208:209], v208 offset:1024\n\t"
    "v_cvt_pk_bf16_f32 v126, v196, v197\n\t"
    "v_mfma_f32_16x16x32_bf16 a[48:51], a[180:183], v[132:135], a[48:51]\n\t"
    "v_exp_f32 v198, v74\n\t"
    "v_mfma_f32_16x16x32_bf16 a[52:55], a[188:191], v[132:135], a[52:55]\n\t"
    "ds_read_b64_tr_b16 a[218:219], v207 offset:1024\n\t"
    "v_mfma_f32_16x16x32_bf16 a[56:59], a[196:199], v[132:135], a[56:59]\n\t"
    "v_exp_f32 v199, v75\n\t"
    "v_mfma_f32_16x16x32_bf16 a[60:63], a[204:207], v[132:135], a[60:63]\n\t"
    "buffer_load_dwordx4 v210, s[40:43], s56 offen offset:0 lds\n\t"
    "v_mfma_f32_16x16x32_bf16 v[48:51], a[212:215], v[132:135], v[48:51]\n\t"
    "v_exp_f32 v200, v76\n\t"
    "v_mfma_f32_16x16x32_bf16 v[52:55], a[220:223], v[132:135], v[52:55]\n\t"
    "ds_read_b64_tr_b16 a[216:217], v209 offset:1024\n\t"
    "v_cvt_pk_bf16_f32 v127, v198, v199\n\t"
    "v_mfma_f32_16x16x32_bf16 v[56:59], a[228:231], v[132:135], v[56:59]\n\t"
    "ds_read_b64_tr_b16 a[226:227], v206 offset:1536\n\t"
    "v_mfma_f32_16x16x32_bf16 v[60:63], a[236:239], v[132:135], v[60:63]\n\t"
    "v_exp_f32 v201, v77\n\t"
    "v_mfma_f32_16x16x32_bf16 v[184:187], v[192:195], v[132:135], v[184:187]\n\t"
    "ds_read_b64_tr_b16 a[224:225], v208 offset:1536\n\t"
    "v_cvt_pk_bf16_f32 v124, v200, v201\n\t"
    "v_mfma_f32_16x16x32_bf16 v[80:83], v[140:143], a[128:131], v[108:111]\n\t"
    "v_exp_f32 v202, v78\n\t"
    "v_mfma_f32_16x16x32_bf16 v[80:83], v[144:147], a[132:135], v[80:83]\n\t"
    "buffer_load_dwordx4 v211, s[40:43], s56 offen offset:1024 lds\n\t"
    "v_mfma_f32_16x16x32_bf16 v[80:83], v[148:151], a[136:139], v[80:83]\n\t"
    "v_exp_f32 v203, v79\n\t"
    "v_mfma_f32_16x16x32_bf16 v[80:83], v[152:155], a[140:143], v[80:83]\n\t"
    "ds_read_b64_tr_b16 a[234:235], v207 offset:1536\n\t"
    "v_cvt_pk_bf16_f32 v125, v202, v203\n\t"
    "v_mfma_f32_16x16x32_bf16 v[84:87], v[156:159], a[128:131], v[108:111]\n\t"
    "v_exp_f32 v196, v96\n\t"
    "v_mfma_f32_16x16x32_bf16 v[84:87], v[160:163], a[132:135], v[84:87]\n\t"
    "ds_read_b64_tr_b16 a[232:233], v209 offset:1536\n\t"
    "v_mfma_f32_16x16x32_bf16 v[84:87], v[164:167], a[136:139], v[84:87]\n\t"
    "v_exp_f32 v197, v97\n\t"
    "v_mfma_f32_16x16x32_bf16 v[84:87], v[168:171], a[140:143], v[84:87]\n\t"
    "s_waitcnt lgkmcnt(12)\n\t"
    "v_cvt_pk_bf16_f32 v136, v196, v197\n\t"
    "v_mfma_f32_16x16x32_bf16 a[0:3], a[176:179], v[120:123], a[0:3]\n\t"
    "v_exp_f32 v198, v98\n\t"
    "v_mfma_f32_16x16x32_bf16 a[4:7], a[184:187], v[120:123], a[4:7]\n\t"
    "s_waitcnt lgkmcnt(8)\n\t"
    "buffer_load_dwordx4 v212, s[40:43], s56 offen offset:2048 lds\n\t"
    "v_mfma_f32_16x16x32_bf16 a[8:11], a[192:195], v[120:123], a[8:11]\n\t"
    "v_exp_f32 v199, v99\n\t"
    "v_mfma_f32_16x16x32_bf16 a[12:15], a[200:203], v[120:123], a[12:15]\n\t"
    "s_waitcnt lgkmcnt(4)\n\t"
    "v_cvt_pk_bf16_f32 v137, v198, v199\n\t"
    "v_mfma_f32_16x16x32_bf16 v[0:3], a[208:211], v[120:123], v[0:3]\n\t"
    "v_exp_f32 v200, v80\n\t"
    "v_mfma_f32_16x16x32_bf16 v[4:7], a[216:219], v[120:123], v[4:7]\n\t"
    "s_waitcnt lgkmcnt(0)\n\t"
    "v_mfma_f32_16x16x32_bf16 v[8:11], a[224:227], v[120:123], v[8:11]\n\t"
    "v_exp_f32 v201, v81\n\t"
    "v_mfma_f32_16x16x32_bf16 v[12:15], a[232:235], v[120:123], v[12:15]\n\t"
    "v_cvt_pk_bf16_f32 v130, v200, v201\n\t"
    "v_mfma_f32_16x16x32_bf16 v[172:175], v[192:195], v[120:123], v[172:175]\n\t"
    "v_exp_f32 v202, v82\n\t"
    "v_mfma_f32_16x16x32_bf16 v[88:91], v[140:143], a[144:147], v[112:115]\n\t"
    "ds_read_b128 v[140:143], v204 offset:16384\n\t"
    "v_mfma_f32_16x16x32_bf16 v[88:91], v[144:147], a[148:151], v[88:91]\n\t"
    "v_exp_f32 v203, v83\n\t"
    "v_mfma_f32_16x16x32_bf16 v[88:91], v[148:151], a[152:155], v[88:91]\n\t"
    "ds_read_b128 v[144:147], v204 offset:16896\n\t"
    "v_cvt_pk_bf16_f32 v131, v202, v203\n\t"
    "v_mfma_f32_16x16x32_bf16 v[88:91], v[152:155], a[156:159], v[88:91]\n\t"
    "v_exp_f32 v196, v84\n\t"
    "v_mfma_f32_16x16x32_bf16 v[92:95], v[156:159], a[144:147], v[112:115]\n\t"
    "ds_read_b128 v[148:151], v204 offset:17408\n\t"
    "v_mfma_f32_16x16x32_bf16 v[92:95], v[160:163], a[148:151], v[92:95]\n\t"
    "buffer_load_dwordx4 v213, s[40:43], s56 offen offset:3072 lds\n\t"
    "v_mfma_f32_16x16x32_bf16 v[92:95], v[164:167], a[152:155], v[92:95]\n\t"
    "s_add_u32 s56, s56, 0x4000\n\t"
    "ds_read_b128 v[152:155], v204 offset:17920\n\t"
    "v_mfma_f32_16x16x32_bf16 v[92:95], v[168:171], a[156:159], v[92:95]\n\t"
    "v_exp_f32 v197, v85\n\t"
    "v_mfma_f32_16x16x32_bf16 a[16:19], a[176:179], v[124:127], a[16:19]\n\t"
    "ds_read_b128 v[156:159], v205 offset:16384\n\t"
    "v_cvt_pk_bf16_f32 v128, v196, v197\n\t"
    "v_mfma_f32_16x16x32_bf16 a[20:23], a[184:187], v[124:127], a[20:23]\n\t"
    "v_exp_f32 v198, v86\n\t"
    "v_mfma_f32_16x16x32_bf16 a[24:27], a[192:195], v[124:127], a[24:27]\n\t"
    "ds_read_b128 v[160:163], v205 offset:16896\n\t"
    "v_mfma_f32_16x16x32_bf16 a[28:31], a[200:203], v[124:127], a[28:31]\n\t"
    "v_exp_f32 v199, v87\n\t"
    "v_mfma_f32_16x16x32_bf16 v[16:19], a[208:211], v[124:127], v[16:19]\n\t"
    "ds_read_b128 v[164:167], v205 offset:17408\n\t"
    "v_cvt_pk_bf16_f32 v129, v198, v199\n\t"
    "v_mfma_f32_16x16x32_bf16 v[20:23], a[216:219], v[124:127], v[20:23]\n\t"
    "v_exp_f32 v196, v88\n\t"
    "v_mfma_f32_16x16x32_bf16 v[24:27], a[224:227], v[124:127], v[24:27]\n\t"
    "ds_read_b128 v[168:171], v205 offset:17920\n\t"
    "v_mfma_f32_16x16x32_bf16 v[28:31], a[232:235], v[124:127], v[28:31]\n\t"
    "v_exp_f32 v197, v89\n\t"
    "v_mfma_f32_16x16x32_bf16 v[176:179], v[192:195], v[124:127], v[176:179]\n\t"
    "s_waitcnt lgkmcnt(5)\n\t"
    "v_cvt_pk_bf16_f32 v134, v196, v197\n\t"
    "Lf_odd_%=:\n\t"
    "v_mfma_f32_16x16x32_bf16 v[64:67], v[140:143], a[96:99], v[100:103]\n\t"
    "s_add_u32 m0, m0, 0x10000\n\t"
    "v_exp_f32 v198, v90\n\t"
    "v_mfma_f32_16x16x32_bf16 v[64:67], v[144:147], a[100:103], v[64:67]\n\t"
    "s_xor_b32 s57, s57, 0x8000\n\t"
    "v_xor_b32 v204, 0x8000, v204\n\t"
    "v_xor_b32 v205, 0x8000, v205\n\t"
    "v_mfma_f32_16x16x32_bf16 v[64:67], v[148:151], a[104:107], v[64:67]\n\t"
    "s_waitcnt lgkmcnt(2)\n\t"
    "v_exp_f32 v199, v91\n\t"
    "v_mfma_f32_16x16x32_bf16 v[64:67], v[152:155], a[108:111], v[64:67]\n\t"
    "v_cvt_pk_bf16_f32 v135, v198, v199\n\t"
    "v_mfma_f32_16x16x32_bf16 v[68:71], v[156:159], a[96:99], v[100:103]\n\t"
    "v_exp_f32 v200, v92\n\t"
    "v_mfma_f32_16x16x32_bf16 v[68:71], v[160:163], a[100:103], v[68:71]\n\t"
    "s_waitcnt vmcnt(8) lgkmcnt(0)\n\t"
    "s_barrier\n\t"
    "v_mfma_f32_16x16x32_bf16 v[68:71], v[164:167], a[104:107], v[68:71]\n\t"
    "v_exp_f32 v201, v93\n\t"
    "v_mfma_f32_16x16x32_bf16 v[68:71], v[168:171], a[108:111], v[68:71]\n\t"
    "buffer_load_dwordx4 v210, s[44:47], s56 offen offset:0 lds\n\t"
    "v_mfma_f32_16x16x32_bf16 a[32:35], a[176:179], v[128:131], a[32:35]\n\t"
    "v_exp_f32 v202, v94\n\t"
    "v_mfma_f32_16x16x32_bf16 a[36:39], a[184:187], v[128:131], a[36:39]\n\t"
    "ds_read_b64_tr_b16 a[180:181], v206 offset:16384\n\t"
    "v_cvt_pk_bf16_f32 v132, v200, v201\n\t"
    "v_mfma_f32_16x16x32_bf16 a[40:43], a[192:195], v[128:131], a[40:43]\n\t"
    "ds_read_b64_tr_b16 a[182:183], v208 offset:16384\n\t"
    "v_mfma_f32_16x16x32_bf16 a[44:47], a[200:203], v[128:131], a[44:47]\n\t"
    "v_exp_f32 v203, v95\n\t"
    "v_mfma_f32_16x16x32_bf16 v[32:35], a[208:211], v[128:131], v[32:35]\n\t"
    "ds_read_b64_tr_b16 a[188:189], v207 offset:16384\n\t"
    "v_cvt_pk_bf16_f32 v133, v202, v203\n\t"
    "v_mfma_f32_16x16x32_bf16 v[36:39], a[216:219], v[128:131], v[36:39]\n\t"
    "v_exp_f32 v196, v64\n\t"
    "v_mfma_f32_16x16x32_bf16 v[40:43], a[224:227], v[128:131], v[40:43]\n\t"
    "ds_read_b64_tr_b16 a[190:191], v209 offset:16384\n\t"
    "v_mfma_f32_16x16x32_bf16 v[44:47], a[232:235], v[128:131], v[44:47]\n\t"
    "v_exp_f32 v197, v65\n\t"
    "v_mfma_f32_16x16x32_bf16 v[180:183], v[192:195], v[128:131], v[180:183]\n\t"
    "buffer_load_dwordx4 v211, s[44:47], s56 offen offset:1024 lds\n\t"
    "v_mfma_f32_16x16x32_bf16 v[72:75], v[140:143], a[112:115], v[104:107]\n\t"
    "v_exp_f32 v198, v66\n\t"
    "v_mfma_f32_16x16x32_bf16 v[72:75], v[144:147], a[116:119], v[72:75]\n\t"
    "ds_read_b64_tr_b16 a[196:197], v206 offset:16896\n\t"
    "v_cvt_pk_bf16_f32 v120, v196, v197\n\t"
    "v_mfma_f32_16x16x32_bf16 v[72:75], v[148:151], a[120:123], v[72:75]\n\t"
    "v_exp_f32 v199, v67\n\t"
    "v_mfma_f32_16x16x32_bf16 v[72:75], v[152:155], a[124:127], v[72:75]\n\t"
    "ds_read_b64_tr_b16 a[198:199], v208 offset:16896\n\t"
    "v_cvt_pk_bf16_f32 v121, v198, v199\n\t"
    "v_mfma_f32_16x16x32_bf16 v[76:79], v[156:159], a[112:115], v[104:107]\n\t"
    "v_exp_f32 v200, v68\n\t"
    "v_mfma_f32_16x16x32_bf16 v[76:79], v[160:163], a[116:119], v[76:79]\n\t"
    "ds_read_b64_tr_b16 a[204:205], v207 offset:16896\n\t"
    "v_mfma_f32_16x16x32_bf16 v[76:79], v[164:167], a[120:123], v[76:79]\n\t"
    "v_exp_f32 v201, v69\n\t"
    "v_mfma_f32_16x16x32_bf16 v[76:79], v[168:171], a[124:127], v[76:79]\n\t"
    "ds_read_b64_tr_b16 a[206:207], v209 offset:16896\n\t"
    "v_cvt_pk_bf16_f32 v122, v200, v201\n\t"
    "v_mfma_f32_16x16x32_bf16 v[96:99], v[140:143], a[160:163], v[116:119]\n\t"
    "v_exp_f32 v202, v70\n\t"
    "v_mfma_f32_16x16x32_bf16 v[96:99], v[144:147], a[164:167], v[96:99]\n\t"
    "buffer_load_dwordx4 v212, s[44:47], s56 offen offset:2048 lds\n\t"
    "v_mfma_f32_16x16x32_bf16 v[96:99], v[148:151], a[168:171], v[96:99]\n\t"
    "v_exp_f32 v203, v71\n\t"
    "v_mfma_f32_16x16x32_bf16 v[96:99], v[152:155], a[172:175], v[96:99]\n\t"
    "ds_read_b64_tr_b16 a[212:213], v206 offset:17408\n\t"
    "v_cvt_pk_bf16_f32 v123, v202, v203\n\t"
    "v_mfma_f32_16x16x32_bf16 a[48:51], a[176:179], v[132:135], a[48:51]\n\t"
    "v_exp_f32 v196, v72\n\t"
    "v_mfma_f32_16x16x32_bf16 a[52:55], a[184:187], v[132:135], a[52:55]\n\t"
    "ds_read_b64_tr_b16 a[214:215], v208 offset:17408\n\t"
    "v_mfma_f32_16x16x32_bf16 a[56:59], a[192:195], v[132:135], a[56:59]\n\t"
    "ds_read_b64_tr_b16 a[220:221], v207 offset:17408\n\t"
    "v_mfma_f32_16x16x32_bf16 a[60:63], a[200:203], v[132:135], a[60:63]\n\t"
    "v_exp_f32 v197, v73\n\t"
    "v_mfma_f32_16x16x32_bf16 v[48:51], a[208:211], v[132:135], v[48:51]\n\t"
    "ds_read_b64_tr_b16 a[222:223], v209 offset:17408\n\t"
    "v_cvt_pk_bf16_f32 v124, v196, v197\n\t"
    "v_mfma_f32_16x16x32_bf16 v[52:55], a[216:219], v[132:135], v[52:55]\n\t"
    "v_exp_f32 v198, v74\n\t"
    "v_mfma_f32_16x16x32_bf16 v[56:59], a[224:227], v[132:135], v[56:59]\n\t"
    "ds_read_b64_tr_b16 a[228:229], v206 offset:17920\n\t"
    "v_mfma_f32_16x16x32_bf16 v[60:63], a[232:235], v[132:135], v[60:63]\n\t"
    "v_exp_f32 v199, v75\n\t"
    "v_mfma_f32_16x16x32_bf16 v[184:187], v[192:195], v[132:135], v[184:187]\n\t"
    "buffer_load_dwordx4 v213, s[44:47], s56 offen offset:3072 lds\n\t"
    "v_mfma_f32_16x16x32_bf16 v[80:83], v[140:143], a[128:131], v[108:111]\n\t"
    "s_mov_b32 m0, s57\n\t"
    "v_exp_f32 v200, v76\n\t"
    "v_mfma_f32_16x16x32_bf16 v[80:83], v[144:147], a[132:135], v[80:83]\n\t"
    "ds_read_b64_tr_b16 a[230:231], v208 offset:17920\n\t"
    "v_cvt_pk_bf16_f32 v125, v198, v199\n\t"
    "v_mfma_f32_16x16x32_bf16 v[80:83], v[148:151], a[136:139], v[80:83]\n\t"
    "v_exp_f32 v201, v77\n\t"
    "v_mfma_f32_16x16x32_bf16 v[80:83], v[152:155], a[140:143], v[80:83]\n\t"
    "ds_read_b64_tr_b16 a[236:237], v207 offset:17920\n\t"
    "v_cvt_pk_bf16_f32 v126, v200, v201\n\t"
    "v_mfma_f32_16x16x32_bf16 v[84:87], v[156:159], a[128:131], v[108:111]\n\t"
    "v_exp_f32 v202, v78\n\t"
    "v_mfma_f32_16x16x32_bf16 v[84:87], v[160:163], a[132:135], v[84:87]\n\t"
    "ds_read_b64_tr_b16 a[238:239], v209 offset:17920\n\t"
    "v_mfma_f32_16x16x32_bf16 v[84:87], v[164:167], a[136:139], v[84:87]\n\t"
    "v_exp_f32 v203, v79\n\t"
    "v_mfma_f32_16x16x32_bf16 v[84:87], v[168:171], a[140:143], v[84:87]\n\t"
    "s_waitcnt lgkmcnt(12)\n\t"
    "v_cvt_pk_bf16_f32 v127, v202, v203\n\t"
    "v_xor_b32 v206, 0x8000, v206\n\t"
    "v_mfma_f32_16x16x32_bf16 a[0:3], a[180:183], v[120:123], a[0:3]\n\t"
    "v_exp_f32 v196, v96\n\t"
    "v_mfma_f32_16x16x32_bf16 a[4:7], a[188:191], v[120:123], a[4:7]\n\t"
    "s_waitcnt lgkmcnt(8)\n\t"
    "buffer_load_dwordx4 v210, s[40:43], s56 offen offset:0 lds\n\t"
    "v_mfma_f32_16x16x32_bf16 a[8:11], a[196:199], v[120:123], a[8:11]\n\t"
    "v_exp_f32 v197, v97\n\t"
    "v_mfma_f32_16x16x32_bf16 a[12:15], a[204:207], v[120:123], a[12:15]\n\t"
    "s_waitcnt lgkmcnt(4)\n\t"
    "v_cvt_pk_bf16_f32 v138, v196, v197\n\t"
    "v_xor_b32 v207, 0x8000, v207\n\t"
    "v_mfma_f32_16x16x32_bf16 v[0:3], a[212:215], v[120:123], v[0:3]\n\t"
    "v_xor_b32 v208, 0x8000, v208\n\t"
    "v_xor_b32 v209, 0x8000, v209\n\t"
    "v_mfma_f32_16x16x32_bf16 v[4:7], a[220:223], v[120:123], v[4:7]\n\t"
    "s_waitcnt lgkmcnt(0)\n\t"
    "v_exp_f32 v198, v98\n\t"
    "v_mfma_f32_16x16x32_bf16 v[8:11], a[228:231], v[120:123], v[8:11]\n\t"
    "v_mfma_f32_16x16x32_bf16 v[12:15], a[236:239], v[120:123], v[12:15]\n\t"
    "v_exp_f32 v199, v99\n\t"
    "v_mfma_f32_16x16x32_bf16 v[172:175], v[192:195], v[120:123], v[172:175]\n\t"
    "v_cvt_pk_bf16_f32 v139, v198, v199\n\t"
    "v_mfma_f32_16x16x32_bf16 v[88:91], v[140:143], a[144:147], v[112:115]\n\t"
    "ds_read_b128 v[140:143], v204 offset:0\n\t"
    "v_mfma_f32_16x16x32_bf16 v[88:91], v[144:147], a[148:151], v[88:91]\n\t"
    "v_exp_f32 v200, v80\n\t"
    "v_mfma_f32_16x16x32_bf16 v[88:91], v[148:151], a[152:155], v[88:91]\n\t"
    "ds_read_b128 v[144:147], v204 offset:512\n\t"
    "v_mfma_f32_16x16x32_bf16 v[88:91], v[152:155], a[156:159], v[88:91]\n\t"
    "v_exp_f32 v201, v81\n\t"
    "v_mfma_f32_16x16x32_bf16 v[92:95], v[156:159], a[144:147], v[112:115]\n\t"
    "ds_read_b128 v[148:151], v204 offset:1024\n\t"
    "v_cvt_pk_bf16_f32 v128, v200, v201\n\t"
    "v_mfma_f32_16x16x32_bf16 v[92:95], v[160:163], a[148:151], v[92:95]\n\t"
    "v_exp_f32 v202, v82\n\t"
    "v_mfma_f32_16x16x32_bf16 v[92:95], v[164:167], a[152:155], v[92:95]\n\t"
    "ds_read_b128 v[152:155], v204 offset:1536\n\t"
    "v_mfma_f32_16x16x32_bf16 v[92:95], v[168:171], a[156:159], v[92:95]\n\t"
    "v_exp_f32 v203, v83\n\t"
    "v_mfma_f32_16x16x32_bf16 a[16:19], a[180:183], v[124:127], a[16:19]\n\t"
    "ds_read_b128 v[156:159], v205 offset:0\n\t"
    "v_cvt_pk_bf16_f32 v129, v202, v203\n\t"
    "v_mfma_f32_16x16x32_bf16 a[20:23], a[188:191], v[124:127], a[20:23]\n\t"
    "v_exp_f32 v196, v84\n\t"
    "v_mfma_f32_16x16x32_bf16 a[24:27], a[196:199], v[124:127], a[24:27]\n\t"
    "ds_read_b128 v[160:163], v205 offset:512\n\t"
    "v_mfma_f32_16x16x32_bf16 a[28:31], a[204:207], v[124:127], a[28:31]\n\t"
    "v_exp_f32 v197, v85\n\t"
    "v_mfma_f32_16x16x32_bf16 v[16:19], a[212:215], v[124:127], v[16:19]\n\t"
    "ds_read_b128 v[164:167], v205 offset:1024\n\t"
    "v_cvt_pk_bf16_f32 v130, v196, v197\n\t"
    "v_mfma_f32_16x16x32_bf16 v[20:23], a[220:223], v[124:127], v[20:23]\n\t"
    "v_exp_f32 v198, v86\n\t"
    "v_mfma_f32_16x16x32_bf16 v[24:27], a[228:231], v[124:127], v[24:27]\n\t"
    "ds_read_b128 v[168:171], v205 offset:1536\n\t"
    "v_mfma_f32_16x16x32_bf16 v[28:31], a[236:239], v[124:127], v[28:31]\n\t"
    "v_exp_f32 v199, v87\n\t"
    "v_mfma_f32_16x16x32_bf16 v[176:179], v[192:195], v[124:127], v[176:179]\n\t"
    "buffer_load_dwordx4 v211, s[40:43], s56 offen offset:1024 lds\n\t"
    "v_mfma_f32_16x16x32_bf16 a[64:67], a[178:181], v[136:139], a[64:67]\n\t"
    "v_exp_f32 v196, v88\n\t"
    "v_mfma_f32_16x16x32_bf16 a[68:71], a[186:189], v[136:139], a[68:71]\n\t"
    "buffer_load_dwordx4 v212, s[40:43], s56 offen offset:2048 lds\n\t"
    "v_mfma_f32_16x16x32_bf16 a[72:75], a[194:197], v[136:139], a[72:75]\n\t"
    "v_exp_f32 v197, v89\n\t"
    "v_mfma_f32_16x16x32_bf16 a[76:79], a[202:205], v[136:139], a[76:79]\n\t"
    "v_cvt_pk_bf16_f32 v131, v198, v199\n\t"
    "v_cvt_pk_bf16_f32 v132, v196, v197\n\t"
    "v_mfma_f32_16x16x32_bf16 a[80:83], a[210:213], v[136:139], a[80:83]\n\t"
    "v_exp_f32 v198, v90\n\t"
    "v_mfma_f32_16x16x32_bf16 a[84:87], a[218:221], v[136:139], a[84:87]\n\t"
    "buffer_load_dwordx4 v213, s[40:43], s56 offen offset:3072 lds\n\t"
    "v_mfma_f32_16x16x32_bf16 a[88:91], a[226:229], v[136:139], a[88:91]\n\t"
    "s_add_u32 s56, s56, 0x4000\n\t"
    "v_exp_f32 v199, v91\n\t"
    "v_mfma_f32_16x16x32_bf16 a[92:95], a[234:237], v[136:139], a[92:95]\n\t"
    "s_cmp_lg_u32 s56, 0x4c000\n\t"
    "v_cvt_pk_bf16_f32 v133, v198, v199\n\t"
    "v_mfma_f32_16x16x32_bf16 v[188:191], v[192:195], v[136:139], v[188:191]\n\t"
    "s_cbranch_scc1 Lf_even_%=\n\t"
    "s_waitcnt lgkmcnt(0)\n\t"
    "s_waitcnt vmcnt(0)\n\t"
    "v_mfma_f32_16x16x32_bf16 a[32:35], a[180:183], v[128:131], a[32:35]\n\t"
    "ds_write_b128 v214, a[0:3] offset:0\n\t"
    "v_exp_f32 v200, v92\n\t"
    "v_exp_f32 v201, v93\n\t"
    "v_mfma_f32_16x16x32_bf16 a[36:39], a[188:191], v[128:131], a[36:39]\n\t"
    "v_exp_f32 v202, v94\n\t"
    "v_cvt_pk_bf16_f32 v134, v200, v201\n\t"
    "v_mfma_f32_16x16x32_bf16 a[40:43], a[196:199], v[128:131], a[40:43]\n\t"
    "ds_write_b128 v214, a[4:7] offset:1024\n\t"
    "v_exp_f32 v203, v95\n\t"
    "s_nop 0\n\t"
    "v_mfma_f32_16x16x32_bf16 a[44:47], a[204:207], v[128:131], a[44:47]\n\t"
    "v_cvt_pk_bf16_f32 v135, v202, v203\n\t"
    "v_mov_b32 v224, v100\n\t"
    "v_mfma_f32_16x16x32_bf16 v[32:35], a[212:215], v[128:131], v[32:35]\n\t"
    "ds_write_b128 v214, a[8:11] offset:2048\n\t"
    "v_mov_b32 v226, v104\n\t"
    "v_mov_b32 v225, v172\n\t"
    "v_mfma_f32_16x16x32_bf16 v[36:39], a[220:223], v[128:131], v[36:39]\n\t"
    "v_mov_b32 v227, v176\n\t"
    "v_mfma_f32_16x16x32_bf16 v[40:43], a[228:231], v[128:131], v[40:43]\n\t"
    "ds_write_b128 v214, a[12:15] offset:3072\n\t"
    "v_mfma_f32_16x16x32_bf16 v[44:47], a[236:239], v[128:131], v[44:47]\n\t"
    "v_mfma_f32_16x16x32_bf16 v[180:183], v[192:195], v[128:131], v[180:183]\n\t"
    "ds_write_b128 v215, v[224:227] offset:32768\n\t"
    "v_mfma_f32_16x16x32_bf16 a[48:51], a[180:183], v[132:135], a[48:51]\n\t"
    "ds_write_b128 v214, a[16:19] offset:16384\n\t"
    "v_mfma_f32_16x16x32_bf16 a[52:55], a[188:191], v[132:135], a[52:55]\n\t"
    "v_mfma_f32_16x16x32_bf16 a[56:59], a[196:199], v[132:135], a[56:59]\n\t"
    "ds_write_b128 v214, a[20:23] offset:17408\n\t"
    "v_mfma_f32_16x16x32_bf16 a[60:63], a[204:207], v[132:135], a[60:63]\n\t"
    "v_mfma_f32_16x16x32_bf16 v[48:51], a[212:215], v[132:135], v[48:51]\n\t"
    "ds_write_b128 v214, a[24:27] offset:18432\n\t"
    "v_mfma_f32_16x16x32_bf16 v[52:55], a[220:223], v[132:135], v[52:55]\n\t"
    "v_mfma_f32_16x16x32_bf16 v[56:59], a[228:231], v[132:135], v[56:59]\n\t"
    "ds_write_b128 v214, a[28:31] offset:19456\n\t"
    "v_mfma_f32_16x16x32_bf16 v[60:63], a[236:239], v[132:135], v[60:63]\n\t"
    "v_mfma_f32_16x16x32_bf16 v[184:187], v[192:195], v[132:135], v[184:187]\n\t"
    "ds_write_b128 v215, a[64:67] offset:0\n\t"
    "s_waitcnt lgkmcnt(1)\n\t"
    "s_barrier\n\t"
    "ds_read_b128 v[240:243], v217 offset:32768\n\t"
    "ds_read_b128 v[64:67], v216 offset:0\n\t"
    "ds_read_b128 v[68:71], v216 offset:1024\n\t"
    "ds_read_b128 v[72:75], v216 offset:2048\n\t"
    "ds_read_b128 v[76:79], v216 offset:3072\n\t"
    "ds_read_b128 v[80:83], v216 offset:16384\n\t"
    "ds_read_b128 v[84:87], v216 offset:17408\n\t"
    "ds_read_b128 v[88:91], v216 offset:18432\n\t"
    "ds_read_b128 v[92:95], v216 offset:19456\n\t"
    "ds_write_b128 v214, a[32:35] offset:32768\n\t"
    "ds_write_b128 v214, a[36:39] offset:33792\n\t"
    "v_mov_b32 v228, v108\n\t"
    "v_mov_b32 v230, v112\n\t"
    "v_mov_b32 v232, v116\n\t"
    "v_mov_b32 v233, v188\n\t"
    "v_mov_b32 v229, v180\n\t"
    "v_mov_b32 v231, v184\n\t"
    "v_max3_f32 v237, v225, v227, v229\n\t"
    "v_max3_f32 v234, v237, v231, v233\n\t"
    "ds_write_b128 v215, v[228:231] offset:33792\n\t"
    "ds_write_b128 v215, v[232:235] offset:34816\n\t"
    "s_waitcnt lgkmcnt(4)\n\t"
    "v_sub_f32 v253, v240, v224\n\t"
    "v_sub_f32 v255, v242, v226\n\t"
    "v_min_f32 v156, 0, v253\n\t"
    "v_min_f32 v158, 0, v255\n\t"
    "v_min_f32 v157, 0, -v253\n\t"
    "v_min_f32 v159, 0, -v255\n\t"
    "ds_write_b128 v214, a[40:43] offset:34816\n\t"
    "v_exp_f32 v156, v156\n\t"
    "v_exp_f32 v158, v158\n\t"
    "v_exp_f32 v157, v157\n\t"
    "v_exp_f32 v159, v159\n\t"
    "v_mul_f32 v252, v225, v156\n\t"
    "v_mul_f32 v254, v227, v158\n\t"
    "ds_write_b128 v214, a[44:47] offset:35840\n\t"
    "v_fmac_f32 v252, v241, v157\n\t"
    "v_fmac_f32 v254, v243, v159\n\t"
    "v_rcp_f32 v252, v252\n\t"
    "v_rcp_f32 v254, v254\n\t"
    "v_pk_mul_f32 v[156:157], v[156:157], v[252:253] op_sel_hi:[1,0]\n\t"
    "v_pk_mul_f32 v[158:159], v[158:159], v[254:255] op_sel_hi:[1,0]\n\t"
    "ds_write_b128 v214, a[48:51] offset:49152\n\t"
    "v_pk_mul_f32 v[0:1], v[0:1], v[156:157] op_sel_hi:[1,0]\n\t"
    "v_pk_mul_f32 v[2:3], v[2:3], v[156:157] op_sel_hi:[1,0]\n\t"
    "v_pk_mul_f32 v[4:5], v[4:5], v[156:157] op_sel_hi:[1,0]\n\t"
    "v_pk_mul_f32 v[6:7], v[6:7], v[156:157] op_sel_hi:[1,0]\n\t"
    "v_pk_mul_f32 v[8:9], v[8:9], v[156:157] op_sel_hi:[1,0]\n\t"
    "v_pk_mul_f32 v[10:11], v[10:11], v[156:157] op_sel_hi:[1,0]\n\t"
    "ds_write_b128 v214, a[52:55] offset:50176\n\t"
    "v_pk_mul_f32 v[12:13], v[12:13], v[156:157] op_sel_hi:[1,0]\n\t"
    "v_pk_mul_f32 v[14:15], v[14:15], v[156:157] op_sel_hi:[1,0]\n\t"
    "v_pk_fma_f32 v[0:1], v[64:65], v[156:157], v[0:1] op_sel:[0,1,0] op_sel_hi:[1,1,1]\n\t"
    "v_pk_fma_f32 v[2:3], v[66:67], v[156:157], v[2:3] op_sel:[0,1,0] op_sel_hi:[1,1,1]\n\t"
    "v_pk_fma_f32 v[4:5], v[68:69], v[156:157], v[4:5] op_sel:[0,1,0] op_sel_hi:[1,1,1]\n\t"
    "v_pk_fma_f32 v[6:7], v[70:71], v[156:157], v[6:7] op_sel:[0,1,0] op_sel_hi:[1,1,1]\n\t"
    "ds_write_b128 v214, a[56:59] offset:51200\n\t"
    "v_pk_fma_f32 v[8:9], v[72:73], v[156:157], v[8:9] op_sel:[0,1,0] op_sel_hi:[1,1,1]\n\t"
    "v_pk_fma_f32 v[10:11], v[74:75], v[156:157], v[10:11] op_sel:[0,1,0] op_sel_hi:[1,1,1]\n\t"
    "v_pk_fma_f32 v[12:13], v[76:77], v[156:157], v[12:13] op_sel:[0,1,0] op_sel_hi:[1,1,1]\n\t"
    "v_pk_fma_f32 v[14:15], v[78:79], v[156:157], v[14:15] op_sel:[0,1,0] op_sel_hi:[1,1,1]\n\t"
    "v_cvt_pk_bf16_f32 v100, v0, v1\n\t"
    "v_cvt_pk_bf16_f32 v101, v2, v3\n\t"
    "ds_write_b128 v214, a[60:63] offset:52224\n\t"
    "v_cvt_pk_bf16_f32 v102, v4, v5\n\t"
    "v_cvt_pk_bf16_f32 v103, v6, v7\n\t"
    "s_nop 0\n\t"
    "v_permlane16_swap_b32 v100, v102\n\t"
    "v_permlane16_swap_b32 v101, v103\n\t"
    "buffer_store_dwordx4 v[100:103], v221, s[52:55], 0 offen offset:0 sc1\n\t"
    "ds_write_b128 v215, a[68:71] offset:1024\n\t"
    "v_cvt_pk_bf16_f32 v104, v8, v9\n\t"
    "v_cvt_pk_bf16_f32 v105, v10, v11\n\t"
    "v_cvt_pk_bf16_f32 v106, v12, v13\n\t"
    "v_cvt_pk_bf16_f32 v107, v14, v15\n\t"
    "s_nop 0\n\t"
    "v_permlane16_swap_b32 v104, v106\n\t"
    "ds_write_b128 v215, a[72:75] offset:2048\n\t"
    "v_permlane16_swap_b32 v105, v107\n\t"
    "buffer_store_dwordx4 v[104:107], v221, s[52:55], 0 offen offset:64 sc1\n\t"
    "v_pk_mul_f32 v[16:17], v[16:17], v[158:159] op_sel_hi:[1,0]\n\t"
    "v_pk_mul_f32 v[18:19], v[18:19], v[158:159] op_sel_hi:[1,0]\n\t"
    "v_pk_mul_f32 v[20:21], v[20:21], v[158:159] op_sel_hi:[1,0]\n\t"
    "v_pk_mul_f32 v[22:23], v[22:23], v[158:159] op_sel_hi:[1,0]\n\t"
    "ds_write_b128 v215, a[76:79] offset:3072\n\t"
    "v_pk_mul_f32 v[24:25], v[24:25], v[158:159] op_sel_hi:[1,0]\n\t"
    "v_pk_mul_f32 v[26:27], v[26:27], v[158:159] op_sel_hi:[1,0]\n\t"
    "v_pk_mul_f32 v[28:29], v[28:29], v[158:159] op_sel_hi:[1,0]\n\t"
    "v_pk_mul_f32 v[30:31], v[30:31], v[158:159] op_sel_hi:[1,0]\n\t"
    "v_pk_fma_f32 v[16:17], v[80:81], v[158:159], v[16:17] op_sel:[0,1,0] op_sel_hi:[1,1,1]\n\t"
    "v_pk_fma_f32 v[18:19], v[82:83], v[158:159], v[18:19] op_sel:[0,1,0] op_sel_hi:[1,1,1]\n\t"
    "ds_write_b128 v215, a[80:83] offset:16384\n\t"
    "v_pk_fma_f32 v[20:21], v[84:85], v[158:159], v[20:21] op_sel:[0,1,0] op_sel_hi:[1,1,1]\n\t"
    "v_pk_fma_f32 v[22:23], v[86:87], v[158:159], v[22:23] op_sel:[0,1,0] op_sel_hi:[1,1,1]\n\t"
    "v_pk_fma_f32 v[24:25], v[88:89], v[158:159], v[24:25] op_sel:[0,1,0] op_sel_hi:[1,1,1]\n\t"
    "v_pk_fma_f32 v[26:27], v[90:91], v[158:159], v[26:27] op_sel:[0,1,0] op_sel_hi:[1,1,1]\n\t"
    "v_pk_fma_f32 v[28:29], v[92:93], v[158:159], v[28:29] op_sel:[0,1,0] op_sel_hi:[1,1,1]\n\t"
    "v_pk_fma_f32 v[30:31], v[94:95], v[158:159], v[30:31] op_sel:[0,1,0] op_sel_hi:[1,1,1]\n\t"
    "ds_write_b128 v215, a[84:87] offset:17408\n\t"
    "v_cvt_pk_bf16_f32 v108, v16, v17\n\t"
    "v_cvt_pk_bf16_f32 v109, v18, v19\n\t"
    "v_cvt_pk_bf16_f32 v110, v20, v21\n\t"
    "v_cvt_pk_bf16_f32 v111, v22, v23\n\t"
    "s_nop 0\n\t"
    "v_permlane16_swap_b32 v108, v110\n\t"
    "ds_write_b128 v215, a[88:91] offset:18432\n\t"
    "v_permlane16_swap_b32 v109, v111\n\t"
    "buffer_store_dwordx4 v[108:111], v221, s[52:55], s81 offen offset:0 sc1\n\t"
    "v_cvt_pk_bf16_f32 v112, v24, v25\n\t"
    "v_cvt_pk_bf16_f32 v113, v26, v27\n\t"
    "v_cvt_pk_bf16_f32 v114, v28, v29\n\t"
    "v_cvt_pk_bf16_f32 v115, v30, v31\n\t"
    "ds_write_b128 v215, a[92:95] offset:19456\n\t"
    "s_waitcnt lgkmcnt(0)\n\t"
    "s_barrier\n\t"
    "ds_read_b128 v[244:247], v217 offset:33792\n\t"
    "ds_read_b128 v[140:143], v220 offset:0\n\t"
    "ds_read_b128 v[144:147], v220 offset:4096\n\t"
    "ds_read_b128 v[148:151], v220 offset:8192\n\t"
    "ds_read_b128 v[152:155], v220 offset:12288\n\t"
    "s_nop 0\n\t"
    "v_permlane16_swap_b32 v112, v114\n\t"
    "v_permlane16_swap_b32 v113, v115\n\t"
    "buffer_store_dwordx4 v[112:115], v221, s[52:55], s81 offen offset:64 sc1\n\t"
    "ds_read_b128 v[64:67], v216 offset:32768\n\t"
    "ds_read_b128 v[68:71], v216 offset:33792\n\t"
    "ds_read_b128 v[72:75], v216 offset:34816\n\t"
    "ds_read_b128 v[76:79], v216 offset:35840\n\t"
    "ds_read_b128 v[80:83], v216 offset:49152\n\t"
    "ds_read_b128 v[84:87], v216 offset:50176\n\t"
    "ds_read_b128 v[88:91], v216 offset:51200\n\t"
    "ds_read_b128 v[92:95], v216 offset:52224\n\t"
    "s_waitcnt lgkmcnt(8)\n\t"
    "v_max3_f32 v248, v142, v146, v150\n\t"
    "v_max_f32 v248, v248, v154\n\t"
    "v_cmp_lt_f32 vcc, s58, v248\n\t"
    "v_sub_f32 v253, v244, v228\n\t"
    "v_sub_f32 v255, v246, v230\n\t"
    "v_min_f32 v160, 0, v253\n\t"
    "v_min_f32 v162, 0, v255\n\t"
    "v_min_f32 v161, 0, -v253\n\t"
    "v_min_f32 v163, 0, -v255\n\t"
    "v_exp_f32 v160, v160\n\t"
    "v_exp_f32 v162, v162\n\t"
    "v_exp_f32 v161, v161\n\t"
    "v_exp_f32 v163, v163\n\t"
    "v_mul_f32 v252, v229, v160\n\t"
    "v_mul_f32 v254, v231, v162\n\t"
    "v_fmac_f32 v252, v245, v161\n\t"
    "v_fmac_f32 v254, v247, v163\n\t"
    "v_rcp_f32 v252, v252\n\t"
    "v_rcp_f32 v254, v254\n\t"
    "v_pk_mul_f32 v[160:161], v[160:161], v[252:253] op_sel_hi:[1,0]\n\t"
    "v_pk_mul_f32 v[162:163], v[162:163], v[254:255] op_sel_hi:[1,0]\n\t"
    "s_cbranch_vccnz L_safe_%=\n\t"
    "ds_read_b128 v[0:3], v218 offset:0\n\t"
    "ds_read_b128 v[4:7], v218 offset:1024\n\t"
    "ds_read_b128 v[8:11], v219 offset:4096\n\t"
    "ds_read_b128 v[12:15], v219 offset:5120\n\t"
    "ds_read_b128 v[16:19], v218 offset:8192\n\t"
    "ds_read_b128 v[20:23], v218 offset:9216\n\t"
    "ds_read_b128 v[24:27], v219 offset:12288\n\t"
    "ds_read_b128 v[28:31], v219 offset:13312\n\t"
    "v_pk_mul_f32 v[32:33], v[32:33], v[160:161] op_sel_hi:[1,0]\n\t"
    "v_pk_mul_f32 v[34:35], v[34:35], v[160:161] op_sel_hi:[1,0]\n\t"
    "v_pk_mul_f32 v[36:37], v[36:37], v[160:161] op_sel_hi:[1,0]\n\t"
    "v_pk_mul_f32 v[38:39], v[38:39], v[160:161] op_sel_hi:[1,0]\n\t"
    "v_pk_mul_f32 v[40:41], v[40:41], v[160:161] op_sel_hi:[1,0]\n\t"
    "v_pk_mul_f32 v[42:43], v[42:43], v[160:161] op_sel_hi:[1,0]\n\t"
    "v_pk_mul_f32 v[44:45], v[44:45], v[160:161] op_sel_hi:[1,0]\n\t"
    "v_pk_mul_f32 v[46:47], v[46:47], v[160:161] op_sel_hi:[1,0]\n\t"
    "s_waitcnt lgkmcnt(12)\n\t"
    "v_pk_fma_f32 v[32:33], v[64:65], v[160:161], v[32:33] op_sel:[0,1,0] op_sel_hi:[1,1,1]\n\t"
    "v_pk_fma_f32 v[34:35], v[66:67], v[160:161], v[34:35] op_sel:[0,1,0] op_sel_hi:[1,1,1]\n\t"
    "v_pk_fma_f32 v[36:37], v[68:69], v[160:161], v[36:37] op_sel:[0,1,0] op_sel_hi:[1,1,1]\n\t"
    "v_pk_fma_f32 v[38:39], v[70:71], v[160:161], v[38:39] op_sel:[0,1,0] op_sel_hi:[1,1,1]\n\t"
    "v_pk_fma_f32 v[40:41], v[72:73], v[160:161], v[40:41] op_sel:[0,1,0] op_sel_hi:[1,1,1]\n\t"
    "v_pk_fma_f32 v[42:43], v[74:75], v[160:161], v[42:43] op_sel:[0,1,0] op_sel_hi:[1,1,1]\n\t"
    "v_pk_fma_f32 v[44:45], v[76:77], v[160:161], v[44:45] op_sel:[0,1,0] op_sel_hi:[1,1,1]\n\t"
    "v_pk_fma_f32 v[46:47], v[78:79], v[160:161], v[46:47] op_sel:[0,1,0] op_sel_hi:[1,1,1]\n\t"
    "v_cvt_pk_bf16_f32 v100, v32, v33\n\t"
    "v_cvt_pk_bf16_f32 v101, v34, v35\n\t"
    "v_cvt_pk_bf16_f32 v102, v36, v37\n\t"
    "v_cvt_pk_bf16_f32 v103, v38, v39\n\t"
    "s_nop 0\n\t"
    "v_permlane16_swap_b32 v100, v102\n\t"
    "v_permlane16_swap_b32 v101, v103\n\t"
    "buffer_store_dwordx4 v[100:103], v221, s[52:55], s82 offen offset:0 sc1\n\t"
    "v_cvt_pk_bf16_f32 v104, v40, v41\n\t"
    "v_cvt_pk_bf16_f32 v105, v42, v43\n\t"
    "v_cvt_pk_bf16_f32 v106, v44, v45\n\t"
    "v_cvt_pk_bf16_f32 v107, v46, v47\n\t"
    "s_nop 0\n\t"
    "v_permlane16_swap_b32 v104, v106\n\t"
    "v_permlane16_swap_b32 v105, v107\n\t"
    "buffer_store_dwordx4 v[104:107], v221, s[52:55], s82 offen offset:64 sc1\n\t"
    "v_pk_mul_f32 v[48:49], v[48:49], v[162:163] op_sel_hi:[1,0]\n\t"
    "v_pk_mul_f32 v[50:51], v[50:51], v[162:163] op_sel_hi:[1,0]\n\t"
    "v_pk_mul_f32 v[52:53], v[52:53], v[162:163] op_sel_hi:[1,0]\n\t"
    "v_pk_mul_f32 v[54:55], v[54:55], v[162:163] op_sel_hi:[1,0]\n\t"
    "v_pk_mul_f32 v[56:57], v[56:57], v[162:163] op_sel_hi:[1,0]\n\t"
    "v_pk_mul_f32 v[58:59], v[58:59], v[162:163] op_sel_hi:[1,0]\n\t"
    "v_pk_mul_f32 v[60:61], v[60:61], v[162:163] op_sel_hi:[1,0]\n\t"
    "v_pk_mul_f32 v[62:63], v[62:63], v[162:163] op_sel_hi:[1,0]\n\t"
    "s_waitcnt lgkmcnt(8)\n\t"
    "v_pk_fma_f32 v[48:49], v[80:81], v[162:163], v[48:49] op_sel:[0,1,0] op_sel_hi:[1,1,1]\n\t"
    "v_pk_fma_f32 v[50:51], v[82:83], v[162:163], v[50:51] op_sel:[0,1,0] op_sel_hi:[1,1,1]\n\t"
    "v_pk_fma_f32 v[52:53], v[84:85], v[162:163], v[52:53] op_sel:[0,1,0] op_sel_hi:[1,1,1]\n\t"
    "v_pk_fma_f32 v[54:55], v[86:87], v[162:163], v[54:55] op_sel:[0,1,0] op_sel_hi:[1,1,1]\n\t"
    "v_pk_fma_f32 v[56:57], v[88:89], v[162:163], v[56:57] op_sel:[0,1,0] op_sel_hi:[1,1,1]\n\t"
    "v_pk_fma_f32 v[58:59], v[90:91], v[162:163], v[58:59] op_sel:[0,1,0] op_sel_hi:[1,1,1]\n\t"
    "v_pk_fma_f32 v[60:61], v[92:93], v[162:163], v[60:61] op_sel:[0,1,0] op_sel_hi:[1,1,1]\n\t"
    "v_pk_fma_f32 v[62:63], v[94:95], v[162:163], v[62:63] op_sel:[0,1,0] op_sel_hi:[1,1,1]\n\t"
    "v_cvt_pk_bf16_f32 v108, v48, v49\n\t"
    "v_cvt_pk_bf16_f32 v109, v50, v51\n\t"
    "v_cvt_pk_bf16_f32 v110, v52, v53\n\t"
    "v_cvt_pk_bf16_f32 v111, v54, v55\n\t"
    "s_nop 0\n\t"
    "v_permlane16_swap_b32 v108, v110\n\t"
    "v_permlane16_swap_b32 v109, v111\n\t"
    "buffer_store_dwordx4 v[108:111], v221, s[52:55], s83 offen offset:0 sc1\n\t"
    "v_cvt_pk_bf16_f32 v112, v56, v57\n\t"
    "v_cvt_pk_bf16_f32 v113, v58, v59\n\t"
    "v_cvt_pk_bf16_f32 v114, v60, v61\n\t"
    "v_cvt_pk_bf16_f32 v115, v62, v63\n\t"
    "s_nop 0\n\t"
    "v_permlane16_swap_b32 v112, v114\n\t"
    "v_permlane16_swap_b32 v113, v115\n\t"
    "buffer_store_dwordx4 v[112:115], v221, s[52:55], s83 offen offset:64 sc1\n\t"
    "v_min3_f32 v248, v140, v144, v148\n\t"
    "v_min_f32 v248, v248, v152\n\t"
    "v_sub_f32 v196, v248, v140\n\t"
    "v_sub_f32 v198, v248, v144\n\t"
    "v_sub_f32 v200, v248, v148\n\t"
    "v_sub_f32 v202, v248, v152\n\t"
    "v_exp_f32 v196, v196\n\t"
    "v_exp_f32 v198, v198\n\t"
    "v_exp_f32 v200, v200\n\t"
    "v_exp_f32 v202, v202\n\t"
    "v_mul_f32 v251, v141, v196\n\t"
    "v_fmac_f32 v251, v145, v198\n\t"
    "v_fmac_f32 v251, v149, v200\n\t"
    "v_fmac_f32 v251, v153, v202\n\t"
    "v_rcp_f32 v251, v251\n\t"
    "s_waitcnt lgkmcnt(0)\n\t"
    "v_mul_f32 v196, v196, v251\n\t"
    "v_mul_f32 v198, v198, v251\n\t"
    "v_mul_f32 v200, v200, v251\n\t"
    "v_mul_f32 v202, v202, v251\n\t"
    "v_pk_mul_f32 v[0:1], v[0:1], v[196:197] op_sel_hi:[1,0]\n\t"
    "v_pk_mul_f32 v[2:3], v[2:3], v[196:197] op_sel_hi:[1,0]\n\t"
    "v_pk_mul_f32 v[4:5], v[4:5], v[196:197] op_sel_hi:[1,0]\n\t"
    "v_pk_mul_f32 v[6:7], v[6:7], v[196:197] op_sel_hi:[1,0]\n\t"
    "v_pk_fma_f32 v[0:1], v[8:9], v[198:199], v[0:1] op_sel_hi:[1,0,1]\n\t"
    "v_pk_fma_f32 v[2:3], v[10:11], v[198:199], v[2:3] op_sel_hi:[1,0,1]\n\t"
    "v_pk_fma_f32 v[4:5], v[12:13], v[198:199], v[4:5] op_sel_hi:[1,0,1]\n\t"
    "v_pk_fma_f32 v[6:7], v[14:15], v[198:199], v[6:7] op_sel_hi:[1,0,1]\n\t"
    "v_pk_fma_f32 v[0:1], v[16:17], v[200:201], v[0:1] op_sel_hi:[1,0,1]\n\t"
    "v_pk_fma_f32 v[2:3], v[18:19], v[200:201], v[2:3] op_sel_hi:[1,0,1]\n\t"
    "v_pk_fma_f32 v[4:5], v[20:21], v[200:201], v[4:5] op_sel_hi:[1,0,1]\n\t"
    "v_pk_fma_f32 v[6:7], v[22:23], v[200:201], v[6:7] op_sel_hi:[1,0,1]\n\t"
    "v_pk_fma_f32 v[0:1], v[24:25], v[202:203], v[0:1] op_sel_hi:[1,0,1]\n\t"
    "v_pk_fma_f32 v[2:3], v[26:27], v[202:203], v[2:3] op_sel_hi:[1,0,1]\n\t"
    "v_pk_fma_f32 v[4:5], v[28:29], v[202:203], v[4:5] op_sel_hi:[1,0,1]\n\t"
    "v_pk_fma_f32 v[6:7], v[30:31], v[202:203], v[6:7] op_sel_hi:[1,0,1]\n\t"
    "v_cvt_pk_bf16_f32 v116, v0, v1\n\t"
    "v_cvt_pk_bf16_f32 v117, v2, v3\n\t"
    "v_cvt_pk_bf16_f32 v118, v4, v5\n\t"
    "v_cvt_pk_bf16_f32 v119, v6, v7\n\t"
    "s_nop 1\n\t"
    "v_permlane16_swap_b32 v116, v118\n\t"
    "v_permlane16_swap_b32 v117, v119\n\t"
    "buffer_store_dwordx4 v[116:119], v222, s[52:55], 0 offen offset:0 sc1\n\t"
    "s_endpgm\n\t"
    "L_safe_%=:\n\t"
    "s_waitcnt vmcnt(0) lgkmcnt(0)\n\t"
    "s_barrier\n\t"
    "s_load_dwordx8 s[64:71], %0, 0x0\n\t"
    "v_mbcnt_lo_u32_b32 v255, -1, 0\n\t"
    "v_mbcnt_hi_u32_b32 v255, -1, v255\n\t"
    "s_mov_b32 s74, %2\n\t"
    "s_lshr_b32 s75, s74, 1\n\t"
    "s_and_b32 s76, s74, 1\n\t"
    "s_and_b32 s62, %1, 7\n\t"
    "s_lshl_b32 s62, s62, 3\n\t"
    "s_lshr_b32 s63, %1, 5\n\t"
    "s_add_u32 s62, s62, s63\n\t"
    "s_bfe_u32 s63, %1, 0x20003\n\t"
    "s_lshl2_add_u32 s63, s62, s63\n\t"
    "s_lshl_b32 s62, s62, 18\n\t"
    "s_mul_i32 s63, s63, 36864\n\t"
    "v_bfe_u32 v241, v255, 2, 3\n\t"
    "v_bfe_u32 v242, v255, 4, 1\n\t"
    "v_lshlrev_b32 v242, 1, v242\n\t"
    "v_and_b32 v243, 3, v255\n\t"
    "v_xor_b32 v242, v242, v243\n\t"
    "v_lshrrev_b32 v243, 5, v255\n\t"
    "v_lshl_add_u32 v242, v243, 2, v242\n\t"
    "v_lshlrev_b32 v242, 4, v242\n\t"
    "v_lshl_add_u32 v244, v241, 8, v242\n\t"
    "v_xor_b32 v245, 16, v244\n\t"
    "s_lshl_b32 s57, s74, 12\n\t"
    "s_lshl_b32 s78, s75, 7\n\t"
    "s_sub_u32 s79, 128, s78\n\t"
    "s_add_u32 s78, s78, s57\n\t"
    "s_add_u32 s78, s78, 1024\n\t"
    "s_add_u32 s79, s79, s57\n\t"
    "v_add_u32 v237, s78, v244\n\t"
    "v_add_u32 v238, s79, v244\n\t"
    "v_add_u32 v239, s78, v245\n\t"
    "v_add_u32 v240, s79, v245\n\t"
    "s_lshl_b32 s78, s76, 12\n\t"
    "s_lshl_b32 s79, s75, 14\n\t"
    "s_add_u32 s78, s78, s79\n\t"
    "s_add_u32 s79, s78, 8192\n\t"
    "s_lshl_b32 s77, s75, 4\n\t"
    "v_xor_b32 v246, s77, v244\n\t"
    "s_lshl_b32 s77, s75, 11\n\t"
    "s_lshl_b32 s58, s76, 7\n\t"
    "s_add_u32 s77, s77, s58\n\t"
    "s_add_u32 s77, s77, 32768\n\t"
    "s_mov_b32 s43, 0x20000\n\t"
    "s_mov_b32 s47, 0x20000\n\t"
    "s_mov_b32 s51, 0x20000\n\t"
    "s_mov_b32 s55, 0x20000\n\t"
    "s_mov_b32 s42, 263168\n\t"
    "s_mov_b32 s46, 295936\n\t"
    "s_mov_b32 s50, 36864\n\t"
    "s_mov_b32 s54, 36864\n\t"
    "s_mov_b32 m0, s57\n\t"
    "s_sub_u32 s80, s62, 1024\n\t"
    "s_cselect_b32 s81, -1, 0\n\t"
    "s_sub_u32 s82, s62, 33792\n\t"
    "s_cselect_b32 s83, -1, 0\n\t"
    "s_waitcnt lgkmcnt(0)\n\t"
    "s_add_u32 s40, s66, s80\n\t"
    "s_addc_u32 s41, s67, s81\n\t"
    "s_and_b32 s41, s41, 0xffff\n\t"
    "buffer_load_dwordx4 v237, s[40:43], 0 offen offset:0 lds\n\t"
    "buffer_load_dwordx4 v238, s[40:43], 0 offen offset:1024 lds\n\t"
    "buffer_load_dwordx4 v239, s[40:43], 0 offen offset:2048 lds\n\t"
    "buffer_load_dwordx4 v240, s[40:43], 0 offen offset:3072 lds\n\t"
    "s_add_u32 s48, s64, s63\n\t"
    "s_addc_u32 s49, s65, 0\n\t"
    "s_and_b32 s49, s49, 0xffff\n\t"
    "buffer_load_dwordx4 v[132:135], v244, s[48:51], s78 offen offset:0\n\t"
    "buffer_load_dwordx4 v[136:139], v244, s[48:51], s78 offen offset:128\n\t"
    "buffer_load_dwordx4 v[140:143], v245, s[48:51], s78 offen offset:2048\n\t"
    "buffer_load_dwordx4 v[144:147], v245, s[48:51], s78 offen offset:2176\n\t"
    "buffer_load_dwordx4 v[148:151], v244, s[48:51], s79 offen offset:0\n\t"
    "buffer_load_dwordx4 v[152:155], v244, s[48:51], s79 offen offset:128\n\t"
    "buffer_load_dwordx4 v[156:159], v245, s[48:51], s79 offen offset:2048\n\t"
    "buffer_load_dwordx4 v[160:163], v245, s[48:51], s79 offen offset:2176\n\t"
    "buffer_load_dwordx4 v[164:167], v246, s[48:51], s77 offen offset:0\n\t"
    "s_add_u32 s44, s68, s82\n\t"
    "s_addc_u32 s45, s69, s83\n\t"
    "s_and_b32 s45, s45, 0xffff\n\t"
    "s_add_u32 s52, s70, s63\n\t"
    "s_addc_u32 s53, s71, 0\n\t"
    "s_and_b32 s53, s53, 0xffff\n\t"
    "s_mov_b32 s60, 0x3e0293ee\n\t"
    "s_mov_b32 s61, 0x3e0293ee\n\t"
    "s_mov_b32 s58, 0x4b800000\n\t"
    "v_bfe_u32 v242, v255, 2, 1\n\t"
    "v_bfe_u32 v247, v255, 3, 1\n\t"
    "v_lshl_add_u32 v242, v242, 1, v247\n\t"
    "v_lshrrev_b32 v243, 5, v255\n\t"
    "v_xor_b32 v247, v243, v242\n\t"
    "v_lshlrev_b32 v247, 4, v247\n\t"
    "v_and_b32 v241, 7, v255\n\t"
    "v_lshl_add_u32 v247, v241, 6, v247\n\t"
    "v_bfe_u32 v241, v255, 3, 2\n\t"
    "v_lshl_add_u32 v247, v241, 11, v247\n\t"
    "s_lshl_b32 s62, s76, 13\n\t"
    "v_add_u32 v230, s62, v247\n\t"
    "v_xor_b32 v231, 32, v230\n\t"
    "s_lshl_b32 s63, s75, 14\n\t"
    "s_add_u32 s63, s63, 98304\n\t"
    "s_lshl_b32 s78, s76, 10\n\t"
    "s_add_u32 s79, s63, s78\n\t"
    "v_add_u32 v248, s79, v247\n\t"
    "v_xor_b32 v249, 32, v248\n\t"
    "s_sub_u32 s79, s63, s78\n\t"
    "v_add_u32 v250, s79, v247\n\t"
    "v_xor_b32 v251, 32, v250\n\t"
    "v_lshrrev_b32 v247, 4, v255\n\t"
    "v_sub_u32 v247, 0, v247\n\t"
    "v_and_b32 v247, 3, v247\n\t"
    "v_xor_b32 v247, v247, v242\n\t"
    "v_lshlrev_b32 v247, 4, v247\n\t"
    "v_and_b32 v241, 7, v255\n\t"
    "v_lshl_add_u32 v247, v241, 6, v247\n\t"
    "v_bfe_u32 v241, v255, 3, 1\n\t"
    "v_lshl_add_u32 v247, v241, 11, v247\n\t"
    "s_lshl_b32 s79, s75, 12\n\t"
    "s_add_u32 s79, s79, s62\n\t"
    "v_add_u32 v232, s79, v247\n\t"
    "s_add_u32 s79, s78, 131072\n\t"
    "v_add_u32 v252, s79, v247\n\t"
    "s_sub_u32 s79, 131072, s78\n\t"
    "v_add_u32 v253, s79, v247\n\t"
    "v_bfe_u32 v247, v255, 4, 1\n\t"
    "v_xor_b32 v247, v247, v243\n\t"
    "v_lshlrev_b32 v247, 5, v247\n\t"
    "v_and_b32 v241, 3, v255\n\t"
    "v_lshl_add_u32 v247, v241, 3, v247\n\t"
    "v_bfe_u32 v241, v255, 2, 2\n\t"
    "v_lshl_add_u32 v247, v241, 6, v247\n\t"
    "v_lshl_add_u32 v247, v243, 8, v247\n\t"
    "s_add_u32 s79, s62, 65536\n\t"
    "v_add_u32 v233, s79, v247\n\t"
    "v_xor_b32 v234, 16, v233\n\t"
    "v_bfe_u32 v247, v255, 4, 1\n\t"
    "v_lshl_add_u32 v242, v247, 1, v243\n\t"
    "v_lshlrev_b32 v242, 4, v242\n\t"
    "v_and_b32 v241, 3, v255\n\t"
    "v_lshlrev_b32 v241, 3, v241\n\t"
    "v_xor_b32 v242, v242, v241\n\t"
    "v_lshl_add_u32 v242, v247, 8, v242\n\t"
    "v_bfe_u32 v241, v255, 2, 2\n\t"
    "v_lshl_add_u32 v242, v241, 6, v242\n\t"
    "v_lshl_add_u32 v242, v243, 11, v242\n\t"
    "s_lshl_b32 s63, s75, 12\n\t"
    "s_add_u32 s79, s79, s63\n\t"
    "v_add_u32 v235, s79, v242\n\t"
    "v_xor_b32 v236, 32, v235\n\t"
    "s_lshl_b32 s78, s76, 12\n\t"
    "s_lshl_b32 s79, s75, 14\n\t"
    "s_add_u32 s78, s78, s79\n\t"
    "s_add_u32 s78, s78, 98304\n\t"
    "v_lshl_add_u32 v241, v255, 4, s78\n\t"
    "s_lshl_b32 s78, s74, 10\n\t"
    "s_add_u32 s78, s78, 131072\n\t"
    "v_lshl_add_u32 v242, v255, 4, s78\n\t"
    "v_accvgpr_write_b32 a0, 0\n\t"
    "v_accvgpr_write_b32 a1, 0\n\t"
    "v_accvgpr_write_b32 a2, 0\n\t"
    "v_accvgpr_write_b32 a3, 0\n\t"
    "v_accvgpr_write_b32 a4, 0\n\t"
    "v_accvgpr_write_b32 a5, 0\n\t"
    "v_accvgpr_write_b32 a6, 0\n\t"
    "v_accvgpr_write_b32 a7, 0\n\t"
    "v_accvgpr_write_b32 a8, 0\n\t"
    "v_accvgpr_write_b32 a9, 0\n\t"
    "v_accvgpr_write_b32 a10, 0\n\t"
    "v_accvgpr_write_b32 a11, 0\n\t"
    "v_accvgpr_write_b32 a12, 0\n\t"
    "v_accvgpr_write_b32 a13, 0\n\t"
    "v_accvgpr_write_b32 a14, 0\n\t"
    "v_accvgpr_write_b32 a15, 0\n\t"
    "v_accvgpr_write_b32 a16, 0\n\t"
    "v_accvgpr_write_b32 a17, 0\n\t"
    "v_accvgpr_write_b32 a18, 0\n\t"
    "v_accvgpr_write_b32 a19, 0\n\t"
    "v_accvgpr_write_b32 a20, 0\n\t"
    "v_accvgpr_write_b32 a21, 0\n\t"
    "v_accvgpr_write_b32 a22, 0\n\t"
    "v_accvgpr_write_b32 a23, 0\n\t"
    "v_accvgpr_write_b32 a24, 0\n\t"
    "v_accvgpr_write_b32 a25, 0\n\t"
    "v_accvgpr_write_b32 a26, 0\n\t"
    "v_accvgpr_write_b32 a27, 0\n\t"
    "v_accvgpr_write_b32 a28, 0\n\t"
    "v_accvgpr_write_b32 a29, 0\n\t"
    "v_accvgpr_write_b32 a30, 0\n\t"
    "v_accvgpr_write_b32 a31, 0\n\t"
    "v_accvgpr_write_b32 a32, 0\n\t"
    "v_accvgpr_write_b32 a33, 0\n\t"
    "v_accvgpr_write_b32 a34, 0\n\t"
    "v_accvgpr_write_b32 a35, 0\n\t"
    "v_accvgpr_write_b32 a36, 0\n\t"
    "v_accvgpr_write_b32 a37, 0\n\t"
    "v_accvgpr_write_b32 a38, 0\n\t"
    "v_accvgpr_write_b32 a39, 0\n\t"
    "v_accvgpr_write_b32 a40, 0\n\t"
    "v_accvgpr_write_b32 a41, 0\n\t"
    "v_accvgpr_write_b32 a42, 0\n\t"
    "v_accvgpr_write_b32 a43, 0\n\t"
    "v_accvgpr_write_b32 a44, 0\n\t"
    "v_accvgpr_write_b32 a45, 0\n\t"
    "v_accvgpr_write_b32 a46, 0\n\t"
    "v_accvgpr_write_b32 a47, 0\n\t"
    "v_accvgpr_write_b32 a48, 0\n\t"
    "v_accvgpr_write_b32 a49, 0\n\t"
    "v_accvgpr_write_b32 a50, 0\n\t"
    "v_accvgpr_write_b32 a51, 0\n\t"
    "v_accvgpr_write_b32 a52, 0\n\t"
    "v_accvgpr_write_b32 a53, 0\n\t"
    "v_accvgpr_write_b32 a54, 0\n\t"
    "v_accvgpr_write_b32 a55, 0\n\t"
    "v_accvgpr_write_b32 a56, 0\n\t"
    "v_accvgpr_write_b32 a57, 0\n\t"
    "v_accvgpr_write_b32 a58, 0\n\t"
    "v_accvgpr_write_b32 a59, 0\n\t"
    "v_accvgpr_write_b32 a60, 0\n\t"
    "v_accvgpr_write_b32 a61, 0\n\t"
    "v_accvgpr_write_b32 a62, 0\n\t"
    "v_accvgpr_write_b32 a63, 0\n\t"
    "v_accvgpr_write_b32 a64, 0\n\t"
    "v_accvgpr_write_b32 a65, 0\n\t"
    "v_accvgpr_write_b32 a66, 0\n\t"
    "v_accvgpr_write_b32 a67, 0\n\t"
    "v_accvgpr_write_b32 a68, 0\n\t"
    "v_accvgpr_write_b32 a69, 0\n\t"
    "v_accvgpr_write_b32 a70, 0\n\t"
    "v_accvgpr_write_b32 a71, 0\n\t"
    "v_accvgpr_write_b32 a72, 0\n\t"
    "v_accvgpr_write_b32 a73, 0\n\t"
    "v_accvgpr_write_b32 a74, 0\n\t"
    "v_accvgpr_write_b32 a75, 0\n\t"
    "v_accvgpr_write_b32 a76, 0\n\t"
    "v_accvgpr_write_b32 a77, 0\n\t"
    "v_accvgpr_write_b32 a78, 0\n\t"
    "v_accvgpr_write_b32 a79, 0\n\t"
    "v_accvgpr_write_b32 a80, 0\n\t"
    "v_accvgpr_write_b32 a81, 0\n\t"
    "v_accvgpr_write_b32 a82, 0\n\t"
    "v_accvgpr_write_b32 a83, 0\n\t"
    "v_accvgpr_write_b32 a84, 0\n\t"
    "v_accvgpr_write_b32 a85, 0\n\t"
    "v_accvgpr_write_b32 a86, 0\n\t"
    "v_accvgpr_write_b32 a87, 0\n\t"
    "v_accvgpr_write_b32 a88, 0\n\t"
    "v_accvgpr_write_b32 a89, 0\n\t"
    "v_accvgpr_write_b32 a90, 0\n\t"
    "v_accvgpr_write_b32 a91, 0\n\t"
    "v_accvgpr_write_b32 a92, 0\n\t"
    "v_accvgpr_write_b32 a93, 0\n\t"
    "v_accvgpr_write_b32 a94, 0\n\t"
    "v_accvgpr_write_b32 a95, 0\n\t"
    "v_accvgpr_write_b32 a96, 0\n\t"
    "v_accvgpr_write_b32 a97, 0\n\t"
    "v_accvgpr_write_b32 a98, 0\n\t"
    "v_accvgpr_write_b32 a99, 0\n\t"
    "v_accvgpr_write_b32 a100, 0\n\t"
    "v_accvgpr_write_b32 a101, 0\n\t"
    "v_accvgpr_write_b32 a102, 0\n\t"
    "v_accvgpr_write_b32 a103, 0\n\t"
    "v_accvgpr_write_b32 a104, 0\n\t"
    "v_accvgpr_write_b32 a105, 0\n\t"
    "v_accvgpr_write_b32 a106, 0\n\t"
    "v_accvgpr_write_b32 a107, 0\n\t"
    "v_accvgpr_write_b32 a108, 0\n\t"
    "v_accvgpr_write_b32 a109, 0\n\t"
    "v_accvgpr_write_b32 a110, 0\n\t"
    "v_accvgpr_write_b32 a111, 0\n\t"
    "v_accvgpr_write_b32 a112, 0\n\t"
    "v_accvgpr_write_b32 a113, 0\n\t"
    "v_accvgpr_write_b32 a114, 0\n\t"
    "v_accvgpr_write_b32 a115, 0\n\t"
    "v_accvgpr_write_b32 a116, 0\n\t"
    "v_accvgpr_write_b32 a117, 0\n\t"
    "v_accvgpr_write_b32 a118, 0\n\t"
    "v_accvgpr_write_b32 a119, 0\n\t"
    "v_accvgpr_write_b32 a120, 0\n\t"
    "v_accvgpr_write_b32 a121, 0\n\t"
    "v_accvgpr_write_b32 a122, 0\n\t"
    "v_accvgpr_write_b32 a123, 0\n\t"
    "v_accvgpr_write_b32 a124, 0\n\t"
    "v_accvgpr_write_b32 a125, 0\n\t"
    "v_accvgpr_write_b32 a126, 0\n\t"
    "v_accvgpr_write_b32 a127, 0\n\t"
    "v_accvgpr_write_b32 a128, 0\n\t"
    "v_accvgpr_write_b32 a129, 0\n\t"
    "v_accvgpr_write_b32 a130, 0\n\t"
    "v_accvgpr_write_b32 a131, 0\n\t"
    "v_accvgpr_write_b32 a132, 0\n\t"
    "v_accvgpr_write_b32 a133, 0\n\t"
    "v_accvgpr_write_b32 a134, 0\n\t"
    "v_accvgpr_write_b32 a135, 0\n\t"
    "v_accvgpr_write_b32 a136, 0\n\t"
    "v_accvgpr_write_b32 a137, 0\n\t"
    "v_accvgpr_write_b32 a138, 0\n\t"
    "v_accvgpr_write_b32 a139, 0\n\t"
    "v_accvgpr_write_b32 a140, 0\n\t"
    "v_accvgpr_write_b32 a141, 0\n\t"
    "v_accvgpr_write_b32 a142, 0\n\t"
    "v_accvgpr_write_b32 a143, 0\n\t"
    "v_accvgpr_write_b32 a144, 0\n\t"
    "v_accvgpr_write_b32 a145, 0\n\t"
    "v_accvgpr_write_b32 a146, 0\n\t"
    "v_accvgpr_write_b32 a147, 0\n\t"
    "v_accvgpr_write_b32 a148, 0\n\t"
    "v_accvgpr_write_b32 a149, 0\n\t"
    "v_accvgpr_write_b32 a150, 0\n\t"
    "v_accvgpr_write_b32 a151, 0\n\t"
    "v_accvgpr_write_b32 a152, 0\n\t"
    "v_accvgpr_write_b32 a153, 0\n\t"
    "v_accvgpr_write_b32 a154, 0\n\t"
    "v_accvgpr_write_b32 a155, 0\n\t"
    "v_accvgpr_write_b32 a156, 0\n\t"
    "v_accvgpr_write_b32 a157, 0\n\t"
    "v_accvgpr_write_b32 a158, 0\n\t"
    "v_accvgpr_write_b32 a159, 0\n\t"
    "v_mov_b32 v227, 0\n\t"
    "v_mov_b32 v228, 0\n\t"
    "v_mov_b32 v229, 0\n\t"
    "s_waitcnt vmcnt(9)\n\t"
    "s_barrier\n\t"
    "ds_read_b128 v[100:103], v230 offset:0\n\t"
    "ds_read_b128 v[104:107], v231 offset:0\n\t"
    "ds_read_b128 v[108:111], v230 offset:512\n\t"
    "ds_read_b128 v[112:115], v231 offset:512\n\t"
    "ds_read_b128 v[116:119], v230 offset:1024\n\t"
    "ds_read_b128 v[120:123], v231 offset:1024\n\t"
    "ds_read_b128 v[124:127], v230 offset:1536\n\t"
    "ds_read_b128 v[128:131], v231 offset:1536\n\t"
    "ds_read_b128 v[196:199], v232 offset:0\n\t"
    "ds_read_b128 v[200:203], v232 offset:512\n\t"
    "ds_read_b128 v[204:207], v232 offset:1024\n\t"
    "ds_read_b128 v[208:211], v232 offset:1536\n\t"
    "s_add_u32 m0, s57, 0x4000\n\t"
    "s_mov_b32 s56, 0x4000\n\t"
    "buffer_load_dwordx4 v237, s[40:43], s56 offen offset:0 lds\n\t"
    "buffer_load_dwordx4 v238, s[40:43], s56 offen offset:1024 lds\n\t"
    "buffer_load_dwordx4 v239, s[40:43], s56 offen offset:2048 lds\n\t"
    "buffer_load_dwordx4 v240, s[40:43], s56 offen offset:3072 lds\n\t"
    "s_add_u32 m0, s57, 0x10000\n\t"
    "s_mov_b32 s56, 0x8000\n\t"
    "buffer_load_dwordx4 v237, s[44:47], s56 offen offset:0 lds\n\t"
    "buffer_load_dwordx4 v238, s[44:47], s56 offen offset:1024 lds\n\t"
    "buffer_load_dwordx4 v239, s[44:47], s56 offen offset:2048 lds\n\t"
    "buffer_load_dwordx4 v240, s[44:47], s56 offen offset:3072 lds\n\t"
    "s_waitcnt vmcnt(16)\n\t"
    "v_lshlrev_b32 v80, 16, v132\n\t"
    "v_and_b32 v81, 0xffff0000, v132\n\t"
    "v_lshlrev_b32 v82, 16, v133\n\t"
    "v_and_b32 v83, 0xffff0000, v133\n\t"
    "v_lshlrev_b32 v84, 16, v134\n\t"
    "v_and_b32 v85, 0xffff0000, v134\n\t"
    "v_lshlrev_b32 v86, 16, v135\n\t"
    "v_and_b32 v87, 0xffff0000, v135\n\t"
    "v_pk_mul_f32 v[80:81], v[80:81], s[60:61]\n\t"
    "v_pk_mul_f32 v[82:83], v[82:83], s[60:61]\n\t"
    "v_pk_mul_f32 v[84:85], v[84:85], s[60:61]\n\t"
    "v_pk_mul_f32 v[86:87], v[86:87], s[60:61]\n\t"
    "v_cvt_pk_bf16_f32 v132, v80, v81\n\t"
    "v_cvt_pk_bf16_f32 v133, v82, v83\n\t"
    "v_cvt_pk_bf16_f32 v134, v84, v85\n\t"
    "v_cvt_pk_bf16_f32 v135, v86, v87\n\t"
    "ds_write_b128 v241, v[132:135] offset:0\n\t"
    "s_waitcnt vmcnt(15)\n\t"
    "v_lshlrev_b32 v80, 16, v136\n\t"
    "v_and_b32 v81, 0xffff0000, v136\n\t"
    "v_lshlrev_b32 v82, 16, v137\n\t"
    "v_and_b32 v83, 0xffff0000, v137\n\t"
    "v_lshlrev_b32 v84, 16, v138\n\t"
    "v_and_b32 v85, 0xffff0000, v138\n\t"
    "v_lshlrev_b32 v86, 16, v139\n\t"
    "v_and_b32 v87, 0xffff0000, v139\n\t"
    "v_pk_mul_f32 v[80:81], v[80:81], s[60:61]\n\t"
    "v_pk_mul_f32 v[82:83], v[82:83], s[60:61]\n\t"
    "v_pk_mul_f32 v[84:85], v[84:85], s[60:61]\n\t"
    "v_pk_mul_f32 v[86:87], v[86:87], s[60:61]\n\t"
    "v_cvt_pk_bf16_f32 v136, v80, v81\n\t"
    "v_cvt_pk_bf16_f32 v137, v82, v83\n\t"
    "v_cvt_pk_bf16_f32 v138, v84, v85\n\t"
    "v_cvt_pk_bf16_f32 v139, v86, v87\n\t"
    "ds_write_b128 v241, v[136:139] offset:1024\n\t"
    "s_waitcnt vmcnt(14)\n\t"
    "v_lshlrev_b32 v80, 16, v140\n\t"
    "v_and_b32 v81, 0xffff0000, v140\n\t"
    "v_lshlrev_b32 v82, 16, v141\n\t"
    "v_and_b32 v83, 0xffff0000, v141\n\t"
    "v_lshlrev_b32 v84, 16, v142\n\t"
    "v_and_b32 v85, 0xffff0000, v142\n\t"
    "v_lshlrev_b32 v86, 16, v143\n\t"
    "v_and_b32 v87, 0xffff0000, v143\n\t"
    "v_pk_mul_f32 v[80:81], v[80:81], s[60:61]\n\t"
    "v_pk_mul_f32 v[82:83], v[82:83], s[60:61]\n\t"
    "v_pk_mul_f32 v[84:85], v[84:85], s[60:61]\n\t"
    "v_pk_mul_f32 v[86:87], v[86:87], s[60:61]\n\t"
    "v_cvt_pk_bf16_f32 v140, v80, v81\n\t"
    "v_cvt_pk_bf16_f32 v141, v82, v83\n\t"
    "v_cvt_pk_bf16_f32 v142, v84, v85\n\t"
    "v_cvt_pk_bf16_f32 v143, v86, v87\n\t"
    "ds_write_b128 v241, v[140:143] offset:2048\n\t"
    "s_waitcnt vmcnt(13)\n\t"
    "v_lshlrev_b32 v80, 16, v144\n\t"
    "v_and_b32 v81, 0xffff0000, v144\n\t"
    "v_lshlrev_b32 v82, 16, v145\n\t"
    "v_and_b32 v83, 0xffff0000, v145\n\t"
    "v_lshlrev_b32 v84, 16, v146\n\t"
    "v_and_b32 v85, 0xffff0000, v146\n\t"
    "v_lshlrev_b32 v86, 16, v147\n\t"
    "v_and_b32 v87, 0xffff0000, v147\n\t"
    "v_pk_mul_f32 v[80:81], v[80:81], s[60:61]\n\t"
    "v_pk_mul_f32 v[82:83], v[82:83], s[60:61]\n\t"
    "v_pk_mul_f32 v[84:85], v[84:85], s[60:61]\n\t"
    "v_pk_mul_f32 v[86:87], v[86:87], s[60:61]\n\t"
    "v_cvt_pk_bf16_f32 v144, v80, v81\n\t"
    "v_cvt_pk_bf16_f32 v145, v82, v83\n\t"
    "v_cvt_pk_bf16_f32 v146, v84, v85\n\t"
    "v_cvt_pk_bf16_f32 v147, v86, v87\n\t"
    "ds_write_b128 v241, v[144:147] offset:3072\n\t"
    "s_waitcnt vmcnt(12)\n\t"
    "v_lshlrev_b32 v80, 16, v148\n\t"
    "v_and_b32 v81, 0xffff0000, v148\n\t"
    "v_lshlrev_b32 v82, 16, v149\n\t"
    "v_and_b32 v83, 0xffff0000, v149\n\t"
    "v_lshlrev_b32 v84, 16, v150\n\t"
    "v_and_b32 v85, 0xffff0000, v150\n\t"
    "v_lshlrev_b32 v86, 16, v151\n\t"
    "v_and_b32 v87, 0xffff0000, v151\n\t"
    "v_pk_mul_f32 v[80:81], v[80:81], s[60:61]\n\t"
    "v_pk_mul_f32 v[82:83], v[82:83], s[60:61]\n\t"
    "v_pk_mul_f32 v[84:85], v[84:85], s[60:61]\n\t"
    "v_pk_mul_f32 v[86:87], v[86:87], s[60:61]\n\t"
    "v_cvt_pk_bf16_f32 v148, v80, v81\n\t"
    "v_cvt_pk_bf16_f32 v149, v82, v83\n\t"
    "v_cvt_pk_bf16_f32 v150, v84, v85\n\t"
    "v_cvt_pk_bf16_f32 v151, v86, v87\n\t"
    "s_waitcnt lgkmcnt(0)\n\t"
    "s_barrier\n\t"
    "ds_read_b128 a[160:163], v248 offset:0\n\t"
    "ds_read_b128 a[164:167], v249 offset:0\n\t"
    "ds_read_b128 a[168:171], v248 offset:512\n\t"
    "ds_read_b128 a[172:175], v249 offset:512\n\t"
    "ds_read_b128 a[176:179], v250 offset:1024\n\t"
    "ds_read_b128 a[180:183], v251 offset:1024\n\t"
    "ds_read_b128 a[184:187], v250 offset:1536\n\t"
    "ds_read_b128 a[188:191], v251 offset:1536\n\t"
    "ds_write_b128 v241, v[148:151] offset:8192\n\t"
    "s_waitcnt vmcnt(8)\n\t"
    "v_lshlrev_b32 v80, 16, v152\n\t"
    "v_and_b32 v81, 0xffff0000, v152\n\t"
    "v_lshlrev_b32 v82, 16, v153\n\t"
    "v_and_b32 v83, 0xffff0000, v153\n\t"
    "v_lshlrev_b32 v84, 16, v154\n\t"
    "v_and_b32 v85, 0xffff0000, v154\n\t"
    "v_lshlrev_b32 v86, 16, v155\n\t"
    "v_and_b32 v87, 0xffff0000, v155\n\t"
    "v_pk_mul_f32 v[80:81], v[80:81], s[60:61]\n\t"
    "v_pk_mul_f32 v[82:83], v[82:83], s[60:61]\n\t"
    "v_pk_mul_f32 v[84:85], v[84:85], s[60:61]\n\t"
    "v_pk_mul_f32 v[86:87], v[86:87], s[60:61]\n\t"
    "v_cvt_pk_bf16_f32 v152, v80, v81\n\t"
    "v_cvt_pk_bf16_f32 v153, v82, v83\n\t"
    "v_cvt_pk_bf16_f32 v154, v84, v85\n\t"
    "v_cvt_pk_bf16_f32 v155, v86, v87\n\t"
    "ds_write_b128 v241, v[152:155] offset:9216\n\t"
    "s_waitcnt lgkmcnt(2)\n\t"
    "v_mfma_f32_32x32x16_bf16 v[0:15], v[100:103], a[160:163], 0\n\t"
    "v_lshlrev_b32 v80, 16, v156\n\t"
    "v_and_b32 v81, 0xffff0000, v156\n\t"
    "v_lshlrev_b32 v82, 16, v157\n\t"
    "v_and_b32 v83, 0xffff0000, v157\n\t"
    "v_lshlrev_b32 v84, 16, v158\n\t"
    "v_and_b32 v85, 0xffff0000, v158\n\t"
    "v_lshlrev_b32 v86, 16, v159\n\t"
    "v_and_b32 v87, 0xffff0000, v159\n\t"
    "v_mfma_f32_32x32x16_bf16 v[0:15], v[104:107], a[164:167], v[0:15]\n\t"
    "v_mul_f32 v80, s60, v80\n\t"
    "v_mul_f32 v81, s60, v81\n\t"
    "v_mul_f32 v82, s60, v82\n\t"
    "v_mul_f32 v83, s60, v83\n\t"
    "v_mul_f32 v84, s60, v84\n\t"
    "v_mul_f32 v85, s60, v85\n\t"
    "v_mul_f32 v86, s60, v86\n\t"
    "v_mul_f32 v87, s60, v87\n\t"
    "v_mfma_f32_32x32x16_bf16 v[0:15], v[108:111], a[168:171], v[0:15]\n\t"
    "v_cvt_pk_bf16_f32 v156, v80, v81\n\t"
    "v_cvt_pk_bf16_f32 v157, v82, v83\n\t"
    "v_cvt_pk_bf16_f32 v158, v84, v85\n\t"
    "v_cvt_pk_bf16_f32 v159, v86, v87\n\t"
    "ds_write_b128 v241, v[156:159] offset:10240\n\t"
    "v_lshlrev_b32 v80, 16, v160\n\t"
    "v_and_b32 v81, 0xffff0000, v160\n\t"
    "v_lshlrev_b32 v82, 16, v161\n\t"
    "v_mfma_f32_32x32x16_bf16 v[0:15], v[112:115], a[172:175], v[0:15]\n\t"
    "v_and_b32 v83, 0xffff0000, v161\n\t"
    "v_lshlrev_b32 v84, 16, v162\n\t"
    "v_and_b32 v85, 0xffff0000, v162\n\t"
    "v_lshlrev_b32 v86, 16, v163\n\t"
    "v_and_b32 v87, 0xffff0000, v163\n\t"
    "v_mul_f32 v80, s60, v80\n\t"
    "v_mul_f32 v81, s60, v81\n\t"
    "v_mul_f32 v82, s60, v82\n\t"
    "v_mfma_f32_32x32x16_bf16 v[0:15], v[116:119], a[176:179], v[0:15]\n\t"
    "v_mul_f32 v83, s60, v83\n\t"
    "v_mul_f32 v84, s60, v84\n\t"
    "v_mul_f32 v85, s60, v85\n\t"
    "v_mul_f32 v86, s60, v86\n\t"
    "v_mul_f32 v87, s60, v87\n\t"
    "v_cvt_pk_bf16_f32 v160, v80, v81\n\t"
    "v_cvt_pk_bf16_f32 v161, v82, v83\n\t"
    "v_cvt_pk_bf16_f32 v162, v84, v85\n\t"
    "v_mfma_f32_32x32x16_bf16 v[0:15], v[120:123], a[180:183], v[0:15]\n\t"
    "v_cvt_pk_bf16_f32 v163, v86, v87\n\t"
    "ds_write_b128 v241, v[160:163] offset:11264\n\t"
    "v_lshlrev_b32 v80, 16, v164\n\t"
    "v_and_b32 v81, 0xffff0000, v164\n\t"
    "v_lshlrev_b32 v82, 16, v165\n\t"
    "v_and_b32 v83, 0xffff0000, v165\n\t"
    "v_lshlrev_b32 v84, 16, v166\n\t"
    "v_and_b32 v85, 0xffff0000, v166\n\t"
    "v_mfma_f32_32x32x16_bf16 v[0:15], v[124:127], a[184:187], v[0:15]\n\t"
    "v_lshlrev_b32 v86, 16, v167\n\t"
    "v_and_b32 v87, 0xffff0000, v167\n\t"
    "v_mul_f32 v80, s60, v80\n\t"
    "v_mul_f32 v81, s60, v81\n\t"
    "v_mul_f32 v82, s60, v82\n\t"
    "v_mul_f32 v83, s60, v83\n\t"
    "v_mul_f32 v84, s60, v84\n\t"
    "v_mul_f32 v85, s60, v85\n\t"
    "v_mfma_f32_32x32x16_bf16 v[0:15], v[128:131], a[188:191], v[0:15]\n\t"
    "v_mul_f32 v86, s60, v86\n\t"
    "v_mul_f32 v87, s60, v87\n\t"
    "v_cvt_pk_bf16_f32 v164, v80, v81\n\t"
    "v_cvt_pk_bf16_f32 v165, v82, v83\n\t"
    "v_cvt_pk_bf16_f32 v166, v84, v85\n\t"
    "v_cvt_pk_bf16_f32 v167, v86, v87\n\t"
    "ds_write_b128 v242, v[164:167] offset:0\n\t"
    "s_add_u32 m0, s57, 0x8000\n\t"
    "s_mov_b32 s56, 0x8000\n\t"
    "buffer_load_dwordx4 v237, s[40:43], s56 offen offset:0 lds\n\t"
    "s_waitcnt lgkmcnt(0)\n\t"
    "s_barrier\n\t"
    "ds_read_b128 a[192:195], v248 offset:8192\n\t"
    "ds_read_b128 a[196:199], v249 offset:8192\n\t"
    "ds_read_b128 a[200:203], v248 offset:8704\n\t"
    "ds_read_b128 a[204:207], v249 offset:8704\n\t"
    "ds_read_b128 a[208:211], v250 offset:9216\n\t"
    "ds_read_b128 a[212:215], v251 offset:9216\n\t"
    "ds_read_b128 a[216:219], v250 offset:9728\n\t"
    "ds_read_b128 a[220:223], v251 offset:9728\n\t"
    "ds_read_b128 a[224:227], v252 offset:0\n\t"
    "ds_read_b128 a[228:231], v252 offset:512\n\t"
    "ds_read_b128 a[232:235], v253 offset:1024\n\t"
    "ds_read_b128 a[236:239], v253 offset:1536\n\t"
    "v_max3_f32 v243, v0, v1, v2\n\t"
    "v_max3_f32 v243, v243, v3, v4\n\t"
    "v_max3_f32 v243, v243, v5, v6\n\t"
    "v_max3_f32 v243, v243, v7, v8\n\t"
    "v_max3_f32 v243, v243, v9, v10\n\t"
    "v_max3_f32 v243, v243, v11, v12\n\t"
    "v_max3_f32 v243, v243, v13, v14\n\t"
    "v_max_f32 v243, v243, v15\n\t"
    "v_mov_b32 v244, v243\n\t"
    "s_nop 1\n\t"
    "v_permlane32_swap_b32 v243, v244\n\t"
    "v_max_f32 v243, v243, v244\n\t"
    "v_sub_f32 v0, v0, v243\n\t"
    "v_sub_f32 v1, v1, v243\n\t"
    "s_waitcnt lgkmcnt(10)\n\t"
    "v_mfma_f32_32x32x16_bf16 v[16:31], v[100:103], a[192:195], 0\n\t"
    "buffer_load_dwordx4 v238, s[40:43], s56 offen offset:1024 lds\n\t"
    "v_sub_f32 v2, v2, v243\n\t"
    "v_sub_f32 v3, v3, v243\n\t"
    "v_sub_f32 v4, v4, v243\n\t"
    "v_sub_f32 v5, v5, v243\n\t"
    "v_sub_f32 v6, v6, v243\n\t"
    "v_mfma_f32_32x32x16_bf16 v[16:31], v[104:107], a[196:199], v[16:31]\n\t"
    "buffer_load_dwordx4 v239, s[40:43], s56 offen offset:2048 lds\n\t"
    "v_sub_f32 v7, v7, v243\n\t"
    "v_sub_f32 v8, v8, v243\n\t"
    "v_sub_f32 v9, v9, v243\n\t"
    "v_sub_f32 v10, v10, v243\n\t"
    "v_sub_f32 v11, v11, v243\n\t"
    "s_waitcnt lgkmcnt(8)\n\t"
    "v_mfma_f32_32x32x16_bf16 v[16:31], v[108:111], a[200:203], v[16:31]\n\t"
    "buffer_load_dwordx4 v240, s[40:43], s56 offen offset:3072 lds\n\t"
    "v_sub_f32 v12, v12, v243\n\t"
    "v_sub_f32 v13, v13, v243\n\t"
    "v_sub_f32 v14, v14, v243\n\t"
    "v_sub_f32 v15, v15, v243\n\t"
    "v_sub_f32 v32, 0, v243\n\t"
    "v_mfma_f32_32x32x16_bf16 v[16:31], v[112:115], a[204:207], v[16:31]\n\t"
    "s_add_u32 m0, s57, 0x14000\n\t"
    "s_mov_b32 s56, 0xc000\n\t"
    "buffer_load_dwordx4 v237, s[44:47], s56 offen offset:0 lds\n\t"
    "v_sub_f32 v33, 0, v243\n\t"
    "v_sub_f32 v34, 0, v243\n\t"
    "v_sub_f32 v35, 0, v243\n\t"
    "v_sub_f32 v36, 0, v243\n\t"
    "v_sub_f32 v37, 0, v243\n\t"
    "s_waitcnt lgkmcnt(4)\n\t"
    "v_mfma_f32_32x32x16_bf16 v[16:31], v[116:119], a[208:211], v[16:31]\n\t"
    "buffer_load_dwordx4 v238, s[44:47], s56 offen offset:1024 lds\n\t"
    "v_sub_f32 v38, 0, v243\n\t"
    "v_sub_f32 v39, 0, v243\n\t"
    "v_sub_f32 v40, 0, v243\n\t"
    "v_sub_f32 v41, 0, v243\n\t"
    "v_sub_f32 v42, 0, v243\n\t"
    "v_mfma_f32_32x32x16_bf16 v[16:31], v[120:123], a[212:215], v[16:31]\n\t"
    "buffer_load_dwordx4 v239, s[44:47], s56 offen offset:2048 lds\n\t"
    "v_sub_f32 v43, 0, v243\n\t"
    "v_sub_f32 v44, 0, v243\n\t"
    "v_sub_f32 v45, 0, v243\n\t"
    "v_sub_f32 v46, 0, v243\n\t"
    "v_sub_f32 v47, 0, v243\n\t"
    "v_mfma_f32_32x32x16_bf16 v[16:31], v[124:127], a[216:219], v[16:31]\n\t"
    "buffer_load_dwordx4 v240, s[44:47], s56 offen offset:3072 lds\n\t"
    "v_exp_f32 v212, v0\n\t"
    "v_exp_f32 v213, v1\n\t"
    "v_mfma_f32_32x32x16_bf16 v[16:31], v[128:131], a[220:223], v[16:31]\n\t"
    "s_add_u32 m0, s57, 0xc000\n\t"
    "s_mov_b32 s56, 0xc000\n\t"
    "buffer_load_dwordx4 v237, s[40:43], s56 offen offset:0 lds\n\t"
    "v_exp_f32 v214, v2\n\t"
    "v_add_f32 v224, v212, v213\n\t"
    "v_cvt_pk_bf16_f32 v72, v212, v213\n\t"
    "s_waitcnt lgkmcnt(0)\n\t"
    "v_mfma_f32_16x16x32_bf16 v[64:67], v[196:199], a[224:227], 0\n\t"
    "buffer_load_dwordx4 v238, s[40:43], s56 offen offset:1024 lds\n\t"
    "v_mfma_f32_16x16x32_bf16 v[64:67], v[200:203], a[228:231], v[64:67]\n\t"
    "buffer_load_dwordx4 v239, s[40:43], s56 offen offset:2048 lds\n\t"
    "v_mfma_f32_16x16x32_bf16 v[64:67], v[204:207], a[232:235], v[64:67]\n\t"
    "buffer_load_dwordx4 v240, s[40:43], s56 offen offset:3072 lds\n\t"
    "v_mfma_f32_16x16x32_bf16 v[64:67], v[208:211], a[236:239], v[64:67]\n\t"
    "s_mov_b32 s56, 0x10000\n\t"
    "s_mov_b32 m0, s57\n\t"
    "s_waitcnt vmcnt(16) lgkmcnt(0)\n\t"
    "s_barrier\n\t"
    "ds_read_b128 v[100:103], v230 offset:16384\n\t"
    "ds_read_b128 v[104:107], v231 offset:16384\n\t"
    "ds_read_b128 v[108:111], v230 offset:16896\n\t"
    "ds_read_b128 v[112:115], v231 offset:16896\n\t"
    "ds_read_b128 v[116:119], v230 offset:17408\n\t"
    "ds_read_b128 v[120:123], v231 offset:17408\n\t"
    "ds_read_b128 v[124:127], v230 offset:17920\n\t"
    "ds_read_b128 v[128:131], v231 offset:17920\n\t"
    "ds_read_b128 a[240:243], v232 offset:16384\n\t"
    "ds_read_b128 a[244:247], v232 offset:16896\n\t"
    "ds_read_b128 a[248:251], v232 offset:17408\n\t"
    "ds_read_b128 a[252:255], v232 offset:17920\n\t"
    "buffer_load_dwordx4 v237, s[40:43], s56 offen offset:0 lds\n\t"
    "v_exp_f32 v215, v3\n\t"
    "v_add_f32 v224, v224, v214\n\t"
    "v_exp_f32 v212, v4\n\t"
    "v_add_f32 v224, v224, v215\n\t"
    "v_cvt_pk_bf16_f32 v73, v214, v215\n\t"
    "v_exp_f32 v213, v5\n\t"
    "v_add_f32 v224, v224, v212\n\t"
    "v_exp_f32 v214, v6\n\t"
    "v_add_f32 v224, v224, v213\n\t"
    "v_cvt_pk_bf16_f32 v74, v212, v213\n\t"
    "v_exp_f32 v215, v7\n\t"
    "buffer_load_dwordx4 v238, s[40:43], s56 offen offset:1024 lds\n\t"
    "v_add_f32 v224, v224, v214\n\t"
    "v_exp_f32 v212, v8\n\t"
    "v_add_f32 v224, v224, v215\n\t"
    "v_cvt_pk_bf16_f32 v75, v214, v215\n\t"
    "v_exp_f32 v213, v9\n\t"
    "v_add_f32 v224, v224, v212\n\t"
    "v_exp_f32 v214, v10\n\t"
    "v_add_f32 v224, v224, v213\n\t"
    "v_cvt_pk_bf16_f32 v76, v212, v213\n\t"
    "v_exp_f32 v215, v11\n\t"
    "v_add_f32 v224, v224, v214\n\t"
    "buffer_load_dwordx4 v239, s[40:43], s56 offen offset:2048 lds\n\t"
    "v_exp_f32 v212, v12\n\t"
    "v_add_f32 v224, v224, v215\n\t"
    "v_cvt_pk_bf16_f32 v77, v214, v215\n\t"
    "v_exp_f32 v213, v13\n\t"
    "v_add_f32 v224, v224, v212\n\t"
    "v_exp_f32 v214, v14\n\t"
    "v_add_f32 v224, v224, v213\n\t"
    "v_cvt_pk_bf16_f32 v78, v212, v213\n\t"
    "v_exp_f32 v215, v15\n\t"
    "v_add_f32 v224, v224, v214\n\t"
    "s_nop 0\n\t"
    "buffer_load_dwordx4 v240, s[40:43], s56 offen offset:3072 lds\n\t"
    "v_add_f32 v224, v224, v215\n\t"
    "v_cvt_pk_bf16_f32 v79, v214, v215\n\t"
    "v_mov_b32 v227, v224\n\t"
    "v_max3_f32 v243, v16, v17, v18\n\t"
    "v_max3_f32 v243, v243, v19, v20\n\t"
    "v_max3_f32 v243, v243, v21, v22\n\t"
    "v_max3_f32 v243, v243, v23, v24\n\t"
    "v_max3_f32 v243, v243, v25, v26\n\t"
    "v_max3_f32 v243, v243, v27, v28\n\t"
    "v_max3_f32 v243, v243, v29, v30\n\t"
    "v_max_f32 v243, v243, v31\n\t"
    "s_xor_b32 m0, m0, 0x18000\n\t"
    "v_mov_b32 v244, v243\n\t"
    "s_nop 1\n\t"
    "v_permlane32_swap_b32 v243, v244\n\t"
    "v_max_f32 v243, v243, v244\n\t"
    "v_sub_f32 v48, 0, v243\n\t"
    "v_sub_f32 v49, 0, v243\n\t"
    "v_pk_add_f32 v[16:17], v[16:17], v[48:49]\n\t"
    "v_pk_add_f32 v[18:19], v[18:19], v[48:49]\n\t"
    "v_pk_add_f32 v[20:21], v[20:21], v[48:49]\n\t"
    "v_pk_add_f32 v[22:23], v[22:23], v[48:49]\n\t"
    "v_pk_add_f32 v[24:25], v[24:25], v[48:49]\n\t"
    "buffer_load_dwordx4 v237, s[44:47], s56 offen offset:0 lds\n\t"
    "v_pk_add_f32 v[26:27], v[26:27], v[48:49]\n\t"
    "v_pk_add_f32 v[28:29], v[28:29], v[48:49]\n\t"
    "v_pk_add_f32 v[30:31], v[30:31], v[48:49]\n\t"
    "v_pk_mov_b32 v[50:51], v[48:49], v[48:49]\n\t"
    "v_pk_mov_b32 v[52:53], v[48:49], v[48:49]\n\t"
    "v_pk_mov_b32 v[54:55], v[48:49], v[48:49]\n\t"
    "v_pk_mov_b32 v[56:57], v[48:49], v[48:49]\n\t"
    "v_pk_mov_b32 v[58:59], v[48:49], v[48:49]\n\t"
    "v_pk_mov_b32 v[60:61], v[48:49], v[48:49]\n\t"
    "v_pk_mov_b32 v[62:63], v[48:49], v[48:49]\n\t"
    "v_max3_f32 v243, v64, v65, v66\n\t"
    "buffer_load_dwordx4 v238, s[44:47], s56 offen offset:1024 lds\n\t"
    "v_max_f32 v243, v243, v67\n\t"
    "v_mov_b32 v244, v243\n\t"
    "s_nop 1\n\t"
    "v_permlane16_swap_b32 v243, v244\n\t"
    "v_max_f32 v243, v243, v244\n\t"
    "v_mov_b32 v244, v243\n\t"
    "s_nop 1\n\t"
    "v_permlane32_swap_b32 v243, v244\n\t"
    "v_max_f32 v243, v243, v244\n\t"
    "v_sub_f32 v64, v64, v243\n\t"
    "v_sub_f32 v65, v65, v243\n\t"
    "buffer_load_dwordx4 v239, s[44:47], s56 offen offset:2048 lds\n\t"
    "v_sub_f32 v66, v66, v243\n\t"
    "v_sub_f32 v67, v67, v243\n\t"
    "v_sub_f32 v68, 0, v243\n\t"
    "v_sub_f32 v69, 0, v243\n\t"
    "v_sub_f32 v70, 0, v243\n\t"
    "v_sub_f32 v71, 0, v243\n\t"
    "v_exp_f32 v216, v16\n\t"
    "v_exp_f32 v217, v17\n\t"
    "v_exp_f32 v218, v18\n\t"
    "v_add_f32 v225, v216, v217\n\t"
    "v_cvt_pk_bf16_f32 v88, v216, v217\n\t"
    "buffer_load_dwordx4 v240, s[44:47], s56 offen offset:3072 lds\n\t"
    "v_exp_f32 v219, v19\n\t"
    "v_add_f32 v225, v225, v218\n\t"
    "v_exp_f32 v220, v20\n\t"
    "v_add_f32 v225, v225, v219\n\t"
    "v_cvt_pk_bf16_f32 v89, v218, v219\n\t"
    "v_exp_f32 v221, v21\n\t"
    "v_add_f32 v225, v225, v220\n\t"
    "v_exp_f32 v222, v22\n\t"
    "v_add_f32 v225, v225, v221\n\t"
    "v_cvt_pk_bf16_f32 v90, v220, v221\n\t"
    "v_exp_f32 v223, v23\n\t"
    "v_add_f32 v225, v225, v222\n\t"
    "v_exp_f32 v216, v24\n\t"
    "v_add_f32 v225, v225, v223\n\t"
    "v_cvt_pk_bf16_f32 v91, v222, v223\n\t"
    "v_xor_b32 v230, 0x8000, v230\n\t"
    "v_xor_b32 v231, 0x8000, v231\n\t"
    "v_xor_b32 v232, 0x8000, v232\n\t"
    "s_add_u32 s56, s56, 0x4000\n\t"
    "s_add_u32 m0, s57, 0x4000\n\t"
    "s_waitcnt vmcnt(16) lgkmcnt(0)\n\t"
    "s_barrier\n\t"
    "s_branch Ls_odd_%=\n\t"
    ".p2align 6\n\t"
    "Ls_even_%=:\n\t"
    "v_exp_f32 v216, v24\n\t"
    "v_add_f32 v225, v225, v222\n\t"
    "v_add_f32 v225, v225, v223\n\t"
    "v_cvt_pk_bf16_f32 v91, v222, v223\n\t"
    "v_mfma_f32_32x32x16_bf16 v[0:15], v[100:103], a[160:163], v[32:47]\n\t"
    "buffer_load_dwordx4 v237, s[40:43], s56 offen offset:0 lds\n\t"
    "ds_read_b64_tr_b16 v[132:133], v233 offset:16384\n\t"
    "ds_read_b64_tr_b16 v[134:135], v234 offset:18432\n\t"
    "v_add_f32 v225, v225, v216\n\t"
    "v_exp_f32 v217, v25\n\t"
    "v_mfma_f32_32x32x16_bf16 v[0:15], v[104:107], a[164:167], v[0:15]\n\t"
    "ds_read_b64_tr_b16 v[140:141], v233 offset:16896\n\t"
    "ds_read_b64_tr_b16 v[142:143], v234 offset:18944\n\t"
    "v_add_f32 v225, v225, v217\n\t"
    "v_cvt_pk_bf16_f32 v92, v216, v217\n\t"
    "v_exp_f32 v218, v26\n\t"
    "v_mfma_f32_32x32x16_bf16 v[0:15], v[108:111], a[168:171], v[0:15]\n\t"
    "ds_read_b64_tr_b16 v[148:149], v233 offset:17408\n\t"
    "ds_read_b64_tr_b16 v[150:151], v234 offset:19456\n\t"
    "ds_read_b64_tr_b16 v[156:157], v233 offset:17920\n\t"
    "v_add_f32 v225, v225, v218\n\t"
    "v_exp_f32 v219, v27\n\t"
    "v_mfma_f32_32x32x16_bf16 v[0:15], v[112:115], a[172:175], v[0:15]\n\t"
    "buffer_load_dwordx4 v238, s[40:43], s56 offen offset:1024 lds\n\t"
    "ds_read_b64_tr_b16 v[158:159], v234 offset:19968\n\t"
    "v_add_f32 v225, v225, v219\n\t"
    "v_cvt_pk_bf16_f32 v93, v218, v219\n\t"
    "v_exp_f32 v220, v28\n\t"
    "v_mfma_f32_32x32x16_bf16 v[0:15], v[116:119], a[176:179], v[0:15]\n\t"
    "ds_read_b64_tr_b16 v[136:137], v233 offset:20480\n\t"
    "ds_read_b64_tr_b16 v[138:139], v234 offset:22528\n\t"
    "ds_read_b64_tr_b16 v[144:145], v233 offset:20992\n\t"
    "v_add_f32 v225, v225, v220\n\t"
    "v_exp_f32 v221, v29\n\t"
    "v_mfma_f32_32x32x16_bf16 v[0:15], v[120:123], a[180:183], v[0:15]\n\t"
    "ds_read_b64_tr_b16 v[146:147], v234 offset:23040\n\t"
    "ds_read_b64_tr_b16 v[152:153], v233 offset:21504\n\t"
    "v_add_f32 v225, v225, v221\n\t"
    "v_cvt_pk_bf16_f32 v94, v220, v221\n\t"
    "v_exp_f32 v222, v30\n\t"
    "v_mfma_f32_32x32x16_bf16 v[0:15], v[124:127], a[184:187], v[0:15]\n\t"
    "buffer_load_dwordx4 v239, s[40:43], s56 offen offset:2048 lds\n\t"
    "ds_read_b64_tr_b16 v[154:155], v234 offset:23552\n\t"
    "ds_read_b64_tr_b16 v[160:161], v233 offset:22016\n\t"
    "v_add_f32 v225, v225, v222\n\t"
    "v_exp_f32 v223, v31\n\t"
    "v_mfma_f32_32x32x16_bf16 v[0:15], v[128:131], a[188:191], v[0:15]\n\t"
    "v_add_f32 v225, v225, v223\n\t"
    "v_cvt_pk_bf16_f32 v95, v222, v223\n\t"
    "v_cmp_lt_f32 vcc, s58, v225\n\t"
    "s_cbranch_vccnz L_rare_c1e_%=\n\t"
    "L_ret_c1e_%=:\n\t"
    "v_exp_f32 v212, v64\n\t"
    "v_mfma_f32_32x32x16_bf16 v[16:31], v[100:103], a[192:195], v[48:63]\n\t"
    "ds_read_b64_tr_b16 v[162:163], v234 offset:24064\n\t"
    "ds_read_b64_tr_b16 v[166:167], v235 offset:16384\n\t"
    "ds_read_b64_tr_b16 v[170:171], v236 offset:16384\n\t"
    "v_add_f32 v228, v228, v225\n\t"
    "v_exp_f32 v213, v65\n\t"
    "v_mfma_f32_32x32x16_bf16 v[16:31], v[104:107], a[196:199], v[16:31]\n\t"
    "buffer_load_dwordx4 v240, s[40:43], s56 offen offset:3072 lds\n\t"
    "ds_read_b64_tr_b16 v[174:175], v235 offset:16896\n\t"
    "v_add_f32 v226, v212, v213\n\t"
    "v_cvt_pk_bf16_f32 v98, v212, v213\n\t"
    "v_exp_f32 v214, v66\n\t"
    "v_mfma_f32_32x32x16_bf16 v[16:31], v[108:111], a[200:203], v[16:31]\n\t"
    "s_xor_b32 m0, m0, 0x18000\n\t"
    "ds_read_b64_tr_b16 v[178:179], v236 offset:16896\n\t"
    "ds_read_b64_tr_b16 v[182:183], v235 offset:17408\n\t"
    "ds_read_b64_tr_b16 v[186:187], v236 offset:17408\n\t"
    "v_add_f32 v226, v226, v214\n\t"
    "v_exp_f32 v215, v67\n\t"
    "v_mfma_f32_32x32x16_bf16 v[16:31], v[112:115], a[204:207], v[16:31]\n\t"
    "ds_read_b64_tr_b16 v[190:191], v235 offset:17920\n\t"
    "ds_read_b64_tr_b16 v[194:195], v236 offset:17920\n\t"
    "v_add_f32 v226, v226, v215\n\t"
    "v_cvt_pk_bf16_f32 v99, v214, v215\n\t"
    "v_exp_f32 v212, v0\n\t"
    "v_mfma_f32_32x32x16_bf16 v[16:31], v[116:119], a[208:211], v[16:31]\n\t"
    "buffer_load_dwordx4 v237, s[44:47], s56 offen offset:0 lds\n\t"
    "ds_read_b128 a[240:243], v232 offset:16384\n\t"
    "ds_read_b128 a[244:247], v232 offset:16896\n\t"
    "v_exp_f32 v213, v1\n\t"
    "v_mfma_f32_32x32x16_bf16 v[16:31], v[120:123], a[212:215], v[16:31]\n\t"
    "ds_read_b128 a[248:251], v232 offset:17408\n\t"
    "ds_read_b128 a[252:255], v232 offset:17920\n\t"
    "v_add_f32 v224, v212, v213\n\t"
    "v_cvt_pk_bf16_f32 v72, v212, v213\n\t"
    "v_exp_f32 v214, v2\n\t"
    "v_mfma_f32_32x32x16_bf16 v[16:31], v[124:127], a[216:219], v[16:31]\n\t"
    "ds_read_b128 v[100:103], v230 offset:16384\n\t"
    "ds_read_b128 v[104:107], v231 offset:16384\n\t"
    "v_add_f32 v224, v224, v214\n\t"
    "v_exp_f32 v215, v3\n\t"
    "v_mfma_f32_32x32x16_bf16 v[16:31], v[128:131], a[220:223], v[16:31]\n\t"
    "s_waitcnt lgkmcnt(14)\n\t"
    "buffer_load_dwordx4 v238, s[44:47], s56 offen offset:1024 lds\n\t"
    "ds_read_b128 v[108:111], v230 offset:16896\n\t"
    "v_add_f32 v224, v224, v215\n\t"
    "v_cvt_pk_bf16_f32 v73, v214, v215\n\t"
    "v_exp_f32 v212, v4\n\t"
    "v_mfma_f32_32x32x16_bf16 a[0:15], v[132:135], v[80:83], a[0:15]\n\t"
    "ds_read_b128 v[112:115], v231 offset:16896\n\t"
    "ds_read_b128 v[116:119], v230 offset:17408\n\t"
    "v_add_f32 v224, v224, v212\n\t"
    "v_exp_f32 v213, v5\n\t"
    "v_mfma_f32_32x32x16_bf16 a[32:47], v[140:143], v[80:83], a[32:47]\n\t"
    "ds_read_b128 v[120:123], v231 offset:17408\n\t"
    "ds_read_b128 v[124:127], v230 offset:17920\n\t"
    "v_add_f32 v224, v224, v213\n\t"
    "v_cvt_pk_bf16_f32 v74, v212, v213\n\t"
    "v_exp_f32 v214, v6\n\t"
    "v_mfma_f32_32x32x16_bf16 a[64:79], v[148:151], v[80:83], a[64:79]\n\t"
    "buffer_load_dwordx4 v239, s[44:47], s56 offen offset:2048 lds\n\t"
    "ds_read_b128 v[128:131], v231 offset:17920\n\t"
    "v_add_f32 v224, v224, v214\n\t"
    "v_exp_f32 v215, v7\n\t"
    "v_mfma_f32_32x32x16_bf16 a[96:111], v[156:159], v[80:83], a[96:111]\n\t"
    "v_add_f32 v224, v224, v215\n\t"
    "v_cvt_pk_bf16_f32 v75, v214, v215\n\t"
    "v_exp_f32 v212, v8\n\t"
    "v_mfma_f32_32x32x16_bf16 a[0:15], v[136:139], v[84:87], a[0:15]\n\t"
    "v_add_f32 v224, v224, v212\n\t"
    "v_exp_f32 v213, v9\n\t"
    "v_mfma_f32_32x32x16_bf16 a[32:47], v[144:147], v[84:87], a[32:47]\n\t"
    "buffer_load_dwordx4 v240, s[44:47], s56 offen offset:3072 lds\n\t"
    "v_add_f32 v224, v224, v213\n\t"
    "v_cvt_pk_bf16_f32 v76, v212, v213\n\t"
    "v_exp_f32 v214, v10\n\t"
    "v_mfma_f32_32x32x16_bf16 a[64:79], v[152:155], v[84:87], a[64:79]\n\t"
    "s_waitcnt vmcnt(16) lgkmcnt(0)\n\t"
    "s_barrier\n\t"
    "v_add_f32 v224, v224, v214\n\t"
    "v_exp_f32 v215, v11\n\t"
    "v_mfma_f32_32x32x16_bf16 a[96:111], v[160:163], v[84:87], a[96:111]\n\t"
    "v_add_f32 v224, v224, v215\n\t"
    "v_cvt_pk_bf16_f32 v77, v214, v215\n\t"
    "v_exp_f32 v212, v12\n\t"
    "v_mfma_f32_32x32x16_bf16 a[16:31], v[132:135], v[88:91], a[16:31]\n\t"
    "s_add_u32 s56, s56, 0x4000\n\t"
    "v_add_f32 v224, v224, v212\n\t"
    "v_exp_f32 v213, v13\n\t"
    "v_mfma_f32_32x32x16_bf16 a[48:63], v[140:143], v[88:91], a[48:63]\n\t"
    "s_add_u32 m0, s57, 0x4000\n\t"
    "v_add_f32 v224, v224, v213\n\t"
    "v_cvt_pk_bf16_f32 v78, v212, v213\n\t"
    "v_exp_f32 v214, v14\n\t"
    "v_mfma_f32_32x32x16_bf16 a[80:95], v[148:151], v[88:91], a[80:95]\n\t"
    "v_add_f32 v224, v224, v214\n\t"
    "v_exp_f32 v215, v15\n\t"
    "v_mfma_f32_32x32x16_bf16 a[112:127], v[156:159], v[88:91], a[112:127]\n\t"
    "v_exp_f32 v216, v16\n\t"
    "v_add_f32 v224, v224, v215\n\t"
    "v_cvt_pk_bf16_f32 v79, v214, v215\n\t"
    "v_max_f32 v246, v224, v226\n\t"
    "v_cmp_lt_f32 vcc, s58, v246\n\t"
    "v_mfma_f32_32x32x16_bf16 a[16:31], v[136:139], v[92:95], a[16:31]\n\t"
    "s_cbranch_vccnz L_rare_c0e_%=\n\t"
    "L_ret_c0e_%=:\n\t"
    "v_add_f32 v227, v227, v224\n\t"
    "v_add_f32 v229, v229, v226\n\t"
    "v_exp_f32 v217, v17\n\t"
    "v_mfma_f32_16x16x32_bf16 v[64:67], v[196:199], a[224:227], v[68:71]\n\t"
    "v_exp_f32 v218, v18\n\t"
    "v_mfma_f32_16x16x32_bf16 v[64:67], v[200:203], a[228:231], v[64:67]\n\t"
    "v_exp_f32 v219, v19\n\t"
    "v_mfma_f32_16x16x32_bf16 v[64:67], v[204:207], a[232:235], v[64:67]\n\t"
    "v_exp_f32 v220, v20\n\t"
    "v_mfma_f32_16x16x32_bf16 v[64:67], v[208:211], a[236:239], v[64:67]\n\t"
    "v_exp_f32 v221, v21\n\t"
    "v_mfma_f32_16x16x32_bf16 a[128:131], v[164:167], v[96:99], a[128:131]\n\t"
    "v_add_f32 v225, v216, v217\n\t"
    "v_cvt_pk_bf16_f32 v88, v216, v217\n\t"
    "v_mfma_f32_16x16x32_bf16 a[132:135], v[168:171], v[96:99], a[132:135]\n\t"
    "v_add_f32 v225, v225, v218\n\t"
    "v_add_f32 v225, v225, v220\n\t"
    "v_mfma_f32_16x16x32_bf16 a[136:139], v[172:175], v[96:99], a[136:139]\n\t"
    "v_add_f32 v225, v225, v219\n\t"
    "v_cvt_pk_bf16_f32 v89, v218, v219\n\t"
    "v_mfma_f32_16x16x32_bf16 a[140:143], v[176:179], v[96:99], a[140:143]\n\t"
    "v_add_f32 v225, v225, v221\n\t"
    "v_cvt_pk_bf16_f32 v90, v220, v221\n\t"
    "v_mfma_f32_16x16x32_bf16 a[144:147], v[180:183], v[96:99], a[144:147]\n\t"
    "v_xor_b32 v230, 0x8000, v230\n\t"
    "v_xor_b32 v231, 0x8000, v231\n\t"
    "v_mfma_f32_16x16x32_bf16 a[148:151], v[184:187], v[96:99], a[148:151]\n\t"
    "v_xor_b32 v232, 0x8000, v232\n\t"
    "v_xor_b32 v233, 0x8000, v233\n\t"
    "v_mfma_f32_16x16x32_bf16 a[152:155], v[188:191], v[96:99], a[152:155]\n\t"
    "v_xor_b32 v234, 0x8000, v234\n\t"
    "v_xor_b32 v235, 0x8000, v235\n\t"
    "v_mfma_f32_16x16x32_bf16 a[156:159], v[192:195], v[96:99], a[156:159]\n\t"
    "v_xor_b32 v236, 0x8000, v236\n\t"
    "v_mfma_f32_32x32x16_bf16 a[48:63], v[144:147], v[92:95], a[48:63]\n\t"
    "v_exp_f32 v222, v22\n\t"
    "v_mfma_f32_32x32x16_bf16 a[80:95], v[152:155], v[92:95], a[80:95]\n\t"
    "v_exp_f32 v223, v23\n\t"
    "v_add_f32 v225, v225, v222\n\t"
    "v_mfma_f32_32x32x16_bf16 a[112:127], v[160:163], v[92:95], a[112:127]\n\t"
    "v_exp_f32 v216, v24\n\t"
    "v_add_f32 v225, v225, v223\n\t"
    "v_cvt_pk_bf16_f32 v91, v222, v223\n\t"
    "Ls_odd_%=:\n\t"
    "v_mfma_f32_32x32x16_bf16 v[0:15], v[100:103], a[160:163], v[32:47]\n\t"
    "buffer_load_dwordx4 v237, s[40:43], s56 offen offset:0 lds\n\t"
    "ds_read_b64_tr_b16 v[132:133], v233 offset:0\n\t"
    "ds_read_b64_tr_b16 v[134:135], v234 offset:2048\n\t"
    "v_add_f32 v225, v225, v216\n\t"
    "v_exp_f32 v217, v25\n\t"
    "v_mfma_f32_32x32x16_bf16 v[0:15], v[104:107], a[164:167], v[0:15]\n\t"
    "ds_read_b64_tr_b16 v[140:141], v233 offset:512\n\t"
    "ds_read_b64_tr_b16 v[142:143], v234 offset:2560\n\t"
    "v_add_f32 v225, v225, v217\n\t"
    "v_cvt_pk_bf16_f32 v92, v216, v217\n\t"
    "v_exp_f32 v218, v26\n\t"
    "v_mfma_f32_32x32x16_bf16 v[0:15], v[108:111], a[168:171], v[0:15]\n\t"
    "ds_read_b64_tr_b16 v[148:149], v233 offset:1024\n\t"
    "ds_read_b64_tr_b16 v[150:151], v234 offset:3072\n\t"
    "ds_read_b64_tr_b16 v[156:157], v233 offset:1536\n\t"
    "v_add_f32 v225, v225, v218\n\t"
    "v_exp_f32 v219, v27\n\t"
    "v_mfma_f32_32x32x16_bf16 v[0:15], v[112:115], a[172:175], v[0:15]\n\t"
    "buffer_load_dwordx4 v238, s[40:43], s56 offen offset:1024 lds\n\t"
    "ds_read_b64_tr_b16 v[158:159], v234 offset:3584\n\t"
    "v_add_f32 v225, v225, v219\n\t"
    "v_cvt_pk_bf16_f32 v93, v218, v219\n\t"
    "v_exp_f32 v220, v28\n\t"
    "v_mfma_f32_32x32x16_bf16 v[0:15], v[116:119], a[176:179], v[0:15]\n\t"
    "ds_read_b64_tr_b16 v[136:137], v233 offset:4096\n\t"
    "ds_read_b64_tr_b16 v[138:139], v234 offset:6144\n\t"
    "ds_read_b64_tr_b16 v[144:145], v233 offset:4608\n\t"
    "v_add_f32 v225, v225, v220\n\t"
    "v_exp_f32 v221, v29\n\t"
    "v_mfma_f32_32x32x16_bf16 v[0:15], v[120:123], a[180:183], v[0:15]\n\t"
    "ds_read_b64_tr_b16 v[146:147], v234 offset:6656\n\t"
    "ds_read_b64_tr_b16 v[152:153], v233 offset:5120\n\t"
    "v_add_f32 v225, v225, v221\n\t"
    "v_cvt_pk_bf16_f32 v94, v220, v221\n\t"
    "v_exp_f32 v222, v30\n\t"
    "v_mfma_f32_32x32x16_bf16 v[0:15], v[124:127], a[184:187], v[0:15]\n\t"
    "buffer_load_dwordx4 v239, s[40:43], s56 offen offset:2048 lds\n\t"
    "ds_read_b64_tr_b16 v[154:155], v234 offset:7168\n\t"
    "ds_read_b64_tr_b16 v[160:161], v233 offset:5632\n\t"
    "v_add_f32 v225, v225, v222\n\t"
    "v_exp_f32 v223, v31\n\t"
    "v_mfma_f32_32x32x16_bf16 v[0:15], v[128:131], a[188:191], v[0:15]\n\t"
    "v_add_f32 v225, v225, v223\n\t"
    "v_cvt_pk_bf16_f32 v95, v222, v223\n\t"
    "v_cmp_lt_f32 vcc, s58, v225\n\t"
    "s_cbranch_vccnz L_rare_c1o_%=\n\t"
    "L_ret_c1o_%=:\n\t"
    "v_exp_f32 v212, v64\n\t"
    "v_mfma_f32_32x32x16_bf16 v[16:31], v[100:103], a[192:195], v[48:63]\n\t"
    "ds_read_b64_tr_b16 v[162:163], v234 offset:7680\n\t"
    "ds_read_b64_tr_b16 v[164:165], v235 offset:0\n\t"
    "ds_read_b64_tr_b16 v[168:169], v236 offset:0\n\t"
    "v_add_f32 v228, v228, v225\n\t"
    "v_exp_f32 v213, v65\n\t"
    "v_mfma_f32_32x32x16_bf16 v[16:31], v[104:107], a[196:199], v[16:31]\n\t"
    "buffer_load_dwordx4 v240, s[40:43], s56 offen offset:3072 lds\n\t"
    "ds_read_b64_tr_b16 v[172:173], v235 offset:512\n\t"
    "v_add_f32 v226, v212, v213\n\t"
    "v_cvt_pk_bf16_f32 v96, v212, v213\n\t"
    "v_exp_f32 v214, v66\n\t"
    "v_mfma_f32_32x32x16_bf16 v[16:31], v[108:111], a[200:203], v[16:31]\n\t"
    "s_xor_b32 m0, m0, 0x18000\n\t"
    "ds_read_b64_tr_b16 v[176:177], v236 offset:512\n\t"
    "ds_read_b64_tr_b16 v[180:181], v235 offset:1024\n\t"
    "ds_read_b64_tr_b16 v[184:185], v236 offset:1024\n\t"
    "v_add_f32 v226, v226, v214\n\t"
    "v_exp_f32 v215, v67\n\t"
    "v_mfma_f32_32x32x16_bf16 v[16:31], v[112:115], a[204:207], v[16:31]\n\t"
    "ds_read_b64_tr_b16 v[188:189], v235 offset:1536\n\t"
    "ds_read_b64_tr_b16 v[192:193], v236 offset:1536\n\t"
    "v_add_f32 v226, v226, v215\n\t"
    "v_cvt_pk_bf16_f32 v97, v214, v215\n\t"
    "v_exp_f32 v212, v0\n\t"
    "v_mfma_f32_32x32x16_bf16 v[16:31], v[116:119], a[208:211], v[16:31]\n\t"
    "buffer_load_dwordx4 v237, s[44:47], s56 offen offset:0 lds\n\t"
    "ds_read_b128 v[196:199], v232 offset:0\n\t"
    "ds_read_b128 v[200:203], v232 offset:512\n\t"
    "v_exp_f32 v213, v1\n\t"
    "v_mfma_f32_32x32x16_bf16 v[16:31], v[120:123], a[212:215], v[16:31]\n\t"
    "ds_read_b128 v[204:207], v232 offset:1024\n\t"
    "ds_read_b128 v[208:211], v232 offset:1536\n\t"
    "v_add_f32 v224, v212, v213\n\t"
    "v_cvt_pk_bf16_f32 v80, v212, v213\n\t"
    "v_exp_f32 v214, v2\n\t"
    "v_mfma_f32_32x32x16_bf16 v[16:31], v[124:127], a[216:219], v[16:31]\n\t"
    "ds_read_b128 v[100:103], v230 offset:0\n\t"
    "ds_read_b128 v[104:107], v231 offset:0\n\t"
    "v_add_f32 v224, v224, v214\n\t"
    "v_exp_f32 v215, v3\n\t"
    "v_mfma_f32_32x32x16_bf16 v[16:31], v[128:131], a[220:223], v[16:31]\n\t"
    "s_waitcnt lgkmcnt(14)\n\t"
    "buffer_load_dwordx4 v238, s[44:47], s56 offen offset:1024 lds\n\t"
    "ds_read_b128 v[108:111], v230 offset:512\n\t"
    "v_add_f32 v224, v224, v215\n\t"
    "v_cvt_pk_bf16_f32 v81, v214, v215\n\t"
    "v_exp_f32 v212, v4\n\t"
    "v_mfma_f32_32x32x16_bf16 a[0:15], v[132:135], v[72:75], a[0:15]\n\t"
    "ds_read_b128 v[112:115], v231 offset:512\n\t"
    "ds_read_b128 v[116:119], v230 offset:1024\n\t"
    "v_add_f32 v224, v224, v212\n\t"
    "v_exp_f32 v213, v5\n\t"
    "v_mfma_f32_32x32x16_bf16 a[32:47], v[140:143], v[72:75], a[32:47]\n\t"
    "ds_read_b128 v[120:123], v231 offset:1024\n\t"
    "ds_read_b128 v[124:127], v230 offset:1536\n\t"
    "v_add_f32 v224, v224, v213\n\t"
    "v_cvt_pk_bf16_f32 v82, v212, v213\n\t"
    "v_exp_f32 v214, v6\n\t"
    "v_mfma_f32_32x32x16_bf16 a[64:79], v[148:151], v[72:75], a[64:79]\n\t"
    "buffer_load_dwordx4 v239, s[44:47], s56 offen offset:2048 lds\n\t"
    "ds_read_b128 v[128:131], v231 offset:1536\n\t"
    "v_add_f32 v224, v224, v214\n\t"
    "v_exp_f32 v215, v7\n\t"
    "v_mfma_f32_32x32x16_bf16 a[96:111], v[156:159], v[72:75], a[96:111]\n\t"
    "v_add_f32 v224, v224, v215\n\t"
    "v_cvt_pk_bf16_f32 v83, v214, v215\n\t"
    "v_exp_f32 v212, v8\n\t"
    "v_mfma_f32_32x32x16_bf16 a[0:15], v[136:139], v[76:79], a[0:15]\n\t"
    "v_add_f32 v224, v224, v212\n\t"
    "v_exp_f32 v213, v9\n\t"
    "v_mfma_f32_32x32x16_bf16 a[32:47], v[144:147], v[76:79], a[32:47]\n\t"
    "buffer_load_dwordx4 v240, s[44:47], s56 offen offset:3072 lds\n\t"
    "v_add_f32 v224, v224, v213\n\t"
    "v_cvt_pk_bf16_f32 v84, v212, v213\n\t"
    "v_exp_f32 v214, v10\n\t"
    "v_mfma_f32_32x32x16_bf16 a[64:79], v[152:155], v[76:79], a[64:79]\n\t"
    "s_waitcnt vmcnt(16) lgkmcnt(0)\n\t"
    "s_barrier\n\t"
    "v_add_f32 v224, v224, v214\n\t"
    "v_exp_f32 v215, v11\n\t"
    "v_mfma_f32_32x32x16_bf16 a[96:111], v[160:163], v[76:79], a[96:111]\n\t"
    "v_add_f32 v224, v224, v215\n\t"
    "v_cvt_pk_bf16_f32 v85, v214, v215\n\t"
    "v_exp_f32 v212, v12\n\t"
    "v_mfma_f32_32x32x16_bf16 a[16:31], v[132:135], v[88:91], a[16:31]\n\t"
    "s_add_u32 s56, s56, 0x4000\n\t"
    "v_add_f32 v224, v224, v212\n\t"
    "v_exp_f32 v213, v13\n\t"
    "v_mfma_f32_32x32x16_bf16 a[48:63], v[140:143], v[88:91], a[48:63]\n\t"
    "s_xor_b32 s57, s57, 0x8000\n\t"
    "v_add_f32 v224, v224, v213\n\t"
    "v_cvt_pk_bf16_f32 v86, v212, v213\n\t"
    "v_exp_f32 v214, v14\n\t"
    "v_mfma_f32_32x32x16_bf16 a[80:95], v[148:151], v[88:91], a[80:95]\n\t"
    "s_mov_b32 m0, s57\n\t"
    "v_add_f32 v224, v224, v214\n\t"
    "v_exp_f32 v215, v15\n\t"
    "v_mfma_f32_32x32x16_bf16 a[112:127], v[156:159], v[88:91], a[112:127]\n\t"
    "s_cmp_lg_u32 s56, 0x50000\n\t"
    "v_exp_f32 v216, v16\n\t"
    "v_add_f32 v224, v224, v215\n\t"
    "v_cvt_pk_bf16_f32 v87, v214, v215\n\t"
    "v_max_f32 v246, v224, v226\n\t"
    "v_cmp_lt_f32 vcc, s58, v246\n\t"
    "v_mfma_f32_32x32x16_bf16 a[16:31], v[136:139], v[92:95], a[16:31]\n\t"
    "s_cbranch_vccnz L_rare_c0o_%=\n\t"
    "L_ret_c0o_%=:\n\t"
    "v_add_f32 v227, v227, v224\n\t"
    "v_add_f32 v229, v229, v226\n\t"
    "v_exp_f32 v217, v17\n\t"
    "v_mfma_f32_16x16x32_bf16 v[64:67], a[240:243], a[224:227], v[68:71]\n\t"
    "v_exp_f32 v218, v18\n\t"
    "v_mfma_f32_16x16x32_bf16 v[64:67], a[244:247], a[228:231], v[64:67]\n\t"
    "v_exp_f32 v219, v19\n\t"
    "v_mfma_f32_16x16x32_bf16 v[64:67], a[248:251], a[232:235], v[64:67]\n\t"
    "v_exp_f32 v220, v20\n\t"
    "v_mfma_f32_16x16x32_bf16 v[64:67], a[252:255], a[236:239], v[64:67]\n\t"
    "v_exp_f32 v221, v21\n\t"
    "v_mfma_f32_32x32x16_bf16 a[48:63], v[144:147], v[92:95], a[48:63]\n\t"
    "v_exp_f32 v222, v22\n\t"
    "v_add_f32 v225, v216, v217\n\t"
    "v_add_f32 v225, v225, v218\n\t"
    "v_add_f32 v225, v225, v219\n\t"
    "v_cvt_pk_bf16_f32 v88, v216, v217\n\t"
    "v_mfma_f32_32x32x16_bf16 a[80:95], v[152:155], v[92:95], a[80:95]\n\t"
    "v_exp_f32 v223, v23\n\t"
    "v_add_f32 v225, v225, v220\n\t"
    "v_add_f32 v225, v225, v221\n\t"
    "v_cvt_pk_bf16_f32 v89, v218, v219\n\t"
    "v_cvt_pk_bf16_f32 v90, v220, v221\n\t"
    "v_mfma_f32_32x32x16_bf16 a[112:127], v[160:163], v[92:95], a[112:127]\n\t"
    "s_cbranch_scc1 Ls_even_%=\n\t"
    "v_exp_f32 v216, v24\n\t"
    "v_add_f32 v225, v225, v222\n\t"
    "v_add_f32 v225, v225, v223\n\t"
    "v_cvt_pk_bf16_f32 v91, v222, v223\n\t"
    "ds_read_b64_tr_b16 v[132:133], v233 offset:16384\n\t"
    "ds_read_b64_tr_b16 v[134:135], v234 offset:18432\n\t"
    "ds_read_b64_tr_b16 v[140:141], v233 offset:16896\n\t"
    "ds_read_b64_tr_b16 v[142:143], v234 offset:18944\n\t"
    "ds_read_b64_tr_b16 v[136:137], v233 offset:20480\n\t"
    "ds_read_b64_tr_b16 v[138:139], v234 offset:22528\n\t"
    "ds_read_b64_tr_b16 v[144:145], v233 offset:20992\n\t"
    "ds_read_b64_tr_b16 v[146:147], v234 offset:23040\n\t"
    "s_waitcnt vmcnt(0)\n\t"
    "ds_read_b64_tr_b16 v[148:149], v233 offset:17408\n\t"
    "ds_read_b64_tr_b16 v[150:151], v234 offset:19456\n\t"
    "ds_read_b64_tr_b16 v[156:157], v233 offset:17920\n\t"
    "ds_read_b64_tr_b16 v[158:159], v234 offset:19968\n\t"
    "ds_read_b64_tr_b16 v[152:153], v233 offset:21504\n\t"
    "ds_read_b64_tr_b16 v[154:155], v234 offset:23552\n\t"
    "ds_read_b64_tr_b16 v[160:161], v233 offset:22016\n\t"
    "ds_read_b64_tr_b16 v[162:163], v234 offset:24064\n\t"
    "ds_read_b64_tr_b16 v[166:167], v235 offset:16384\n\t"
    "ds_read_b64_tr_b16 v[170:171], v236 offset:16384\n\t"
    "ds_read_b64_tr_b16 v[174:175], v235 offset:16896\n\t"
    "ds_read_b64_tr_b16 v[178:179], v236 offset:16896\n\t"
    "ds_read_b64_tr_b16 v[182:183], v235 offset:17408\n\t"
    "ds_read_b64_tr_b16 v[186:187], v236 offset:17408\n\t"
    "ds_read_b64_tr_b16 v[190:191], v235 offset:17920\n\t"
    "ds_read_b64_tr_b16 v[194:195], v236 offset:17920\n\t"
    "v_lshl_add_u32 v110, v255, 4, s57\n\t"
    "v_and_b32 v110, 0x3fff, v110\n\t"
    "v_add_u32 v111, 0x10000, v110\n\t"
    "v_add_f32 v225, v225, v216\n\t"
    "v_exp_f32 v217, v25\n\t"
    "s_waitcnt lgkmcnt(15)\n\t"
    "v_mfma_f32_32x32x16_bf16 a[0:15], v[132:135], v[80:83], a[0:15]\n\t"
    "v_exp_f32 v218, v26\n\t"
    "v_add_f32 v225, v225, v217\n\t"
    "v_cvt_pk_bf16_f32 v92, v216, v217\n\t"
    "v_exp_f32 v219, v27\n\t"
    "s_waitcnt lgkmcnt(15)\n\t"
    "v_mfma_f32_32x32x16_bf16 a[32:47], v[140:143], v[80:83], a[32:47]\n\t"
    "v_add_f32 v225, v225, v218\n\t"
    "v_exp_f32 v220, v28\n\t"
    "v_add_f32 v225, v225, v219\n\t"
    "v_cvt_pk_bf16_f32 v93, v218, v219\n\t"
    "v_exp_f32 v221, v29\n\t"
    "s_waitcnt lgkmcnt(15)\n\t"
    "v_mfma_f32_32x32x16_bf16 a[0:15], v[136:139], v[84:87], a[0:15]\n\t"
    "v_add_f32 v225, v225, v220\n\t"
    "v_exp_f32 v222, v30\n\t"
    "v_add_f32 v225, v225, v221\n\t"
    "v_cvt_pk_bf16_f32 v94, v220, v221\n\t"
    "v_exp_f32 v223, v31\n\t"
    "s_waitcnt lgkmcnt(15)\n\t"
    "v_mfma_f32_32x32x16_bf16 a[32:47], v[144:147], v[84:87], a[32:47]\n\t"
    "v_add_f32 v225, v225, v222\n\t"
    "v_exp_f32 v212, v64\n\t"
    "v_add_f32 v225, v225, v223\n\t"
    "v_cvt_pk_bf16_f32 v95, v222, v223\n\t"
    "v_cmp_lt_f32 vcc, s58, v225\n\t"
    "s_waitcnt lgkmcnt(8)\n\t"
    "v_mfma_f32_32x32x16_bf16 a[64:79], v[148:151], v[80:83], a[64:79]\n\t"
    "s_cbranch_vccnz L_rare_c1t_%=\n\t"
    "L_ret_c1t_%=:\n\t"
    "v_add_f32 v228, v228, v225\n\t"
    "v_exp_f32 v213, v65\n\t"
    "v_exp_f32 v214, v66\n\t"
    "v_mfma_f32_32x32x16_bf16 a[96:111], v[156:159], v[80:83], a[96:111]\n\t"
    "ds_write_b128 v110, a[0:3] offset:0\n\t"
    "v_add_f32 v226, v212, v213\n\t"
    "v_cvt_pk_bf16_f32 v98, v212, v213\n\t"
    "v_exp_f32 v215, v67\n\t"
    "v_add_f32 v226, v226, v214\n\t"
    "v_add_f32 v226, v226, v215\n\t"
    "v_cvt_pk_bf16_f32 v99, v214, v215\n\t"
    "v_mfma_f32_32x32x16_bf16 a[64:79], v[152:155], v[84:87], a[64:79]\n\t"
    "ds_write_b128 v110, a[4:7] offset:1024\n\t"
    "v_cmp_lt_f32 vcc, s58, v226\n\t"
    "s_cbranch_vccnz L_rare_c16t_%=\n\t"
    "L_ret_c16t_%=:\n\t"
    "v_add_f32 v229, v229, v226\n\t"
    "v_mov_b32 v108, v227\n\t"
    "v_mov_b32 v109, v228\n\t"
    "v_mov_b32 v100, v32\n\t"
    "v_mov_b32 v102, v48\n\t"
    "v_mfma_f32_32x32x16_bf16 a[96:111], v[160:163], v[84:87], a[96:111]\n\t"
    "ds_write_b128 v110, a[8:11] offset:2048\n\t"
    "v_permlane32_swap_b32 v227, v108\n\t"
    "v_permlane32_swap_b32 v228, v109\n\t"
    "v_add_f32 v101, v227, v108\n\t"
    "v_add_f32 v103, v228, v109\n\t"
    "v_mov_b32 v108, v229\n\t"
    "v_mov_b32 v104, v68\n\t"
    "s_nop 0\n\t"
    "v_mfma_f32_32x32x16_bf16 a[16:31], v[132:135], v[88:91], a[16:31]\n\t"
    "ds_write_b128 v110, a[12:15] offset:3072\n\t"
    "v_permlane16_swap_b32 v229, v108\n\t"
    "v_add_f32 v229, v229, v108\n\t"
    "v_mov_b32 v108, v229\n\t"
    "s_nop 1\n\t"
    "v_permlane32_swap_b32 v229, v108\n\t"
    "v_add_f32 v105, v229, v108\n\t"
    "v_mfma_f32_32x32x16_bf16 a[48:63], v[140:143], v[88:91], a[48:63]\n\t"
    "ds_write_b128 v110, a[32:35] offset:16384\n\t"
    "v_max3_f32 v106, v101, v103, v105\n\t"
    "v_xor_b32 v113, 0x1000, v111\n\t"
    "v_lshlrev_b32 v116, 4, v255\n\t"
    "v_add_u32 v116, 0x18400, v116\n\t"
    "v_mfma_f32_32x32x16_bf16 a[16:31], v[136:139], v[92:95], a[16:31]\n\t"
    "ds_write_b128 v110, a[36:39] offset:17408\n\t"
    "v_mfma_f32_32x32x16_bf16 a[48:63], v[144:147], v[92:95], a[48:63]\n\t"
    "ds_write_b128 v110, a[40:43] offset:18432\n\t"
    "s_waitcnt lgkmcnt(7)\n\t"
    "v_mfma_f32_16x16x32_bf16 a[128:131], v[164:167], v[96:99], a[128:131]\n\t"
    "ds_write_b128 v111, v[100:103] offset:32768\n\t"
    "v_mfma_f32_16x16x32_bf16 a[132:135], v[168:171], v[96:99], a[132:135]\n\t"
    "v_xor_b32 v112, 0x1000, v110\n\t"
    "s_lshl_b32 s62, s75, 14\n\t"
    "s_lshl_b32 s63, s76, 11\n\t"
    "s_add_u32 s62, s62, s63\n\t"
    "s_add_u32 s62, s62, 0x10000\n\t"
    "v_lshl_add_u32 v114, v255, 4, s62\n\t"
    "v_xor_b32 v115, 0x4000, v114\n\t"
    "v_mfma_f32_16x16x32_bf16 a[136:139], v[172:175], v[96:99], a[136:139]\n\t"
    "ds_write_b128 v111, v[104:107] offset:33792\n\t"
    "v_mfma_f32_16x16x32_bf16 a[140:143], v[176:179], v[96:99], a[140:143]\n\t"
    "v_and_b32 v118, 15, v255\n\t"
    "v_lshlrev_b32 v118, 8, v118\n\t"
    "v_lshrrev_b32 v108, 5, v255\n\t"
    "v_lshl_add_u32 v118, v108, 4, v118\n\t"
    "v_bfe_u32 v108, v255, 4, 1\n\t"
    "v_lshl_add_u32 v118, v108, 5, v118\n\t"
    "s_lshl_b32 s62, s75, 14\n\t"
    "v_mfma_f32_16x16x32_bf16 a[144:147], v[180:183], v[96:99], a[144:147]\n\t"
    "ds_write_b128 v110, a[44:47] offset:19456\n\t"
    "v_mfma_f32_16x16x32_bf16 a[148:151], v[184:187], v[96:99], a[148:151]\n\t"
    "s_xor_b32 s63, s76, 1\n\t"
    "s_lshl_b32 s63, s63, 7\n\t"
    "s_add_u32 s62, s62, s63\n\t"
    "v_add_u32 v117, s62, v118\n\t"
    "s_lshl_b32 s62, s74, 6\n\t"
    "s_add_u32 s62, s62, 32768\n\t"
    "v_add_u32 v118, s62, v118\n\t"
    "v_mfma_f32_16x16x32_bf16 a[152:155], v[188:191], v[96:99], a[152:155]\n\t"
    "s_waitcnt lgkmcnt(1)\n\t"
    "s_barrier\n\t"
    "ds_read_b128 v[120:123], v113 offset:32768\n\t"
    "ds_read_b128 v[196:199], v116 offset:0\n\t"
    "ds_read_b128 v[200:203], v116 offset:4096\n\t"
    "ds_read_b128 v[204:207], v116 offset:8192\n\t"
    "ds_read_b128 v[208:211], v116 offset:12288\n\t"
    "v_mfma_f32_16x16x32_bf16 a[156:159], v[192:195], v[96:99], a[156:159]\n\t"
    "ds_write_b128 v110, a[16:19] offset:32768\n\t"
    "s_mov_b32 s81, 4096\n\t"
    "s_mov_b32 s82, 8192\n\t"
    "s_mov_b32 s83, 12288\n\t"
    "v_accvgpr_read_b32 v0, a64\n\t"
    "v_accvgpr_read_b32 v1, a65\n\t"
    "v_accvgpr_read_b32 v2, a66\n\t"
    "v_accvgpr_read_b32 v3, a67\n\t"
    "v_mfma_f32_32x32x16_bf16 a[80:95], v[148:151], v[88:91], a[80:95]\n\t"
    "ds_write_b128 v110, a[20:23] offset:33792\n\t"
    "v_accvgpr_read_b32 v4, a68\n\t"
    "v_accvgpr_read_b32 v5, a69\n\t"
    "v_accvgpr_read_b32 v6, a70\n\t"
    "v_accvgpr_read_b32 v7, a71\n\t"
    "v_accvgpr_read_b32 v8, a72\n\t"
    "v_accvgpr_read_b32 v9, a73\n\t"
    "v_accvgpr_read_b32 v10, a74\n\t"
    "v_mfma_f32_32x32x16_bf16 a[112:127], v[156:159], v[88:91], a[112:127]\n\t"
    "ds_write_b128 v110, a[24:27] offset:34816\n\t"
    "v_accvgpr_read_b32 v11, a75\n\t"
    "v_accvgpr_read_b32 v12, a76\n\t"
    "v_accvgpr_read_b32 v13, a77\n\t"
    "v_accvgpr_read_b32 v14, a78\n\t"
    "v_accvgpr_read_b32 v15, a79\n\t"
    "v_accvgpr_read_b32 v16, a96\n\t"
    "v_accvgpr_read_b32 v17, a97\n\t"
    "v_mfma_f32_32x32x16_bf16 a[80:95], v[152:155], v[92:95], a[80:95]\n\t"
    "ds_write_b128 v110, a[28:31] offset:35840\n\t"
    "v_accvgpr_read_b32 v18, a98\n\t"
    "v_accvgpr_read_b32 v19, a99\n\t"
    "v_accvgpr_read_b32 v20, a100\n\t"
    "v_accvgpr_read_b32 v21, a101\n\t"
    "v_accvgpr_read_b32 v22, a102\n\t"
    "v_accvgpr_read_b32 v23, a103\n\t"
    "v_accvgpr_read_b32 v24, a104\n\t"
    "v_mfma_f32_32x32x16_bf16 a[112:127], v[160:163], v[92:95], a[112:127]\n\t"
    "ds_write_b128 v110, a[48:51] offset:49152\n\t"
    "v_accvgpr_read_b32 v25, a105\n\t"
    "v_accvgpr_read_b32 v26, a106\n\t"
    "v_accvgpr_read_b32 v27, a107\n\t"
    "v_accvgpr_read_b32 v28, a108\n\t"
    "v_accvgpr_read_b32 v29, a109\n\t"
    "v_accvgpr_read_b32 v30, a110\n\t"
    "v_accvgpr_read_b32 v31, a111\n\t"
    "s_waitcnt lgkmcnt(5)\n\t"
    "ds_write_b128 v110, a[52:55] offset:50176\n\t"
    "v_min_f32 v241, v100, v120\n\t"
    "v_sub_f32 v242, v241, v100\n\t"
    "v_sub_f32 v243, v241, v120\n\t"
    "v_exp_f32 v242, v242\n\t"
    "v_exp_f32 v243, v243\n\t"
    "s_nop 0\n\t"
    "v_mul_f32 v244, v101, v242\n\t"
    "v_fmac_f32 v244, v121, v243\n\t"
    "v_rcp_f32 v244, v244\n\t"
    "ds_write_b128 v110, a[56:59] offset:51200\n\t"
    "s_nop 0\n\t"
    "v_mul_f32 v124, v242, v244\n\t"
    "v_mul_f32 v126, v243, v244\n\t"
    "v_mul_f32 v125, v242, v244\n\t"
    "v_mul_f32 v127, v243, v244\n\t"
    "v_min_f32 v241, v102, v122\n\t"
    "v_sub_f32 v242, v241, v102\n\t"
    "v_sub_f32 v243, v241, v122\n\t"
    "v_exp_f32 v242, v242\n\t"
    "v_exp_f32 v243, v243\n\t"
    "ds_write_b128 v110, a[60:63] offset:52224\n\t"
    "s_nop 0\n\t"
    "v_mul_f32 v244, v103, v242\n\t"
    "v_fmac_f32 v244, v123, v243\n\t"
    "v_rcp_f32 v244, v244\n\t"
    "s_nop 0\n\t"
    "v_mul_f32 v128, v242, v244\n\t"
    "v_mul_f32 v130, v243, v244\n\t"
    "v_mul_f32 v129, v242, v244\n\t"
    "v_mul_f32 v131, v243, v244\n\t"
    "v_min3_f32 v241, v196, v200, v204\n\t"
    "v_min_f32 v241, v241, v208\n\t"
    "ds_write_b128 v111, a[128:131] offset:0\n\t"
    "v_sub_f32 v64, v241, v196\n\t"
    "v_sub_f32 v66, v241, v200\n\t"
    "v_sub_f32 v68, v241, v204\n\t"
    "v_sub_f32 v70, v241, v208\n\t"
    "v_exp_f32 v64, v64\n\t"
    "v_exp_f32 v66, v66\n\t"
    "v_exp_f32 v68, v68\n\t"
    "v_exp_f32 v70, v70\n\t"
    "ds_write_b128 v111, a[132:135] offset:1024\n\t"
    "v_mul_f32 v244, v197, v64\n\t"
    "v_fmac_f32 v244, v201, v66\n\t"
    "v_fmac_f32 v244, v205, v68\n\t"
    "v_fmac_f32 v244, v209, v70\n\t"
    "v_rcp_f32 v244, v244\n\t"
    "s_nop 0\n\t"
    "v_mul_f32 v65, v64, v244\n\t"
    "v_mul_f32 v64, v64, v244\n\t"
    "v_mul_f32 v67, v66, v244\n\t"
    "v_mul_f32 v66, v66, v244\n\t"
    "v_mul_f32 v69, v68, v244\n\t"
    "ds_write_b128 v111, a[136:139] offset:2048\n\t"
    "v_mul_f32 v68, v68, v244\n\t"
    "v_mul_f32 v71, v70, v244\n\t"
    "v_mul_f32 v70, v70, v244\n\t"
    "v_accvgpr_read_b32 v32, a80\n\t"
    "v_accvgpr_read_b32 v33, a81\n\t"
    "v_accvgpr_read_b32 v34, a82\n\t"
    "v_accvgpr_read_b32 v35, a83\n\t"
    "v_accvgpr_read_b32 v36, a84\n\t"
    "v_accvgpr_read_b32 v37, a85\n\t"
    "v_accvgpr_read_b32 v38, a86\n\t"
    "v_accvgpr_read_b32 v39, a87\n\t"
    "v_accvgpr_read_b32 v40, a88\n\t"
    "ds_write_b128 v111, a[140:143] offset:3072\n\t"
    "v_accvgpr_read_b32 v41, a89\n\t"
    "v_accvgpr_read_b32 v42, a90\n\t"
    "v_accvgpr_read_b32 v43, a91\n\t"
    "v_accvgpr_read_b32 v44, a92\n\t"
    "v_accvgpr_read_b32 v45, a93\n\t"
    "v_accvgpr_read_b32 v46, a94\n\t"
    "v_accvgpr_read_b32 v47, a95\n\t"
    "v_accvgpr_read_b32 v48, a112\n\t"
    "v_accvgpr_read_b32 v49, a113\n\t"
    "v_accvgpr_read_b32 v50, a114\n\t"
    "v_accvgpr_read_b32 v51, a115\n\t"
    "ds_write_b128 v111, a[144:147] offset:16384\n\t"
    "v_accvgpr_read_b32 v52, a116\n\t"
    "v_accvgpr_read_b32 v53, a117\n\t"
    "v_accvgpr_read_b32 v54, a118\n\t"
    "v_accvgpr_read_b32 v55, a119\n\t"
    "v_accvgpr_read_b32 v56, a120\n\t"
    "v_accvgpr_read_b32 v57, a121\n\t"
    "v_accvgpr_read_b32 v58, a122\n\t"
    "v_accvgpr_read_b32 v59, a123\n\t"
    "v_accvgpr_read_b32 v60, a124\n\t"
    "v_accvgpr_read_b32 v61, a125\n\t"
    "v_accvgpr_read_b32 v62, a126\n\t"
    "ds_write_b128 v111, a[148:151] offset:17408\n\t"
    "v_accvgpr_read_b32 v63, a127\n\t"
    "v_pk_mul_f32 v[0:1], v[0:1], v[124:125]\n\t"
    "v_pk_mul_f32 v[2:3], v[2:3], v[124:125]\n\t"
    "v_pk_mul_f32 v[4:5], v[4:5], v[124:125]\n\t"
    "v_pk_mul_f32 v[6:7], v[6:7], v[124:125]\n\t"
    "v_pk_mul_f32 v[8:9], v[8:9], v[124:125]\n\t"
    "v_pk_mul_f32 v[10:11], v[10:11], v[124:125]\n\t"
    "v_pk_mul_f32 v[12:13], v[12:13], v[124:125]\n\t"
    "v_pk_mul_f32 v[14:15], v[14:15], v[124:125]\n\t"
    "v_pk_mul_f32 v[16:17], v[16:17], v[124:125]\n\t"
    "v_pk_mul_f32 v[18:19], v[18:19], v[124:125]\n\t"
    "ds_write_b128 v111, a[152:155] offset:18432\n\t"
    "v_pk_mul_f32 v[20:21], v[20:21], v[124:125]\n\t"
    "v_pk_mul_f32 v[22:23], v[22:23], v[124:125]\n\t"
    "v_pk_mul_f32 v[24:25], v[24:25], v[124:125]\n\t"
    "v_pk_mul_f32 v[26:27], v[26:27], v[124:125]\n\t"
    "v_pk_mul_f32 v[28:29], v[28:29], v[124:125]\n\t"
    "v_pk_mul_f32 v[30:31], v[30:31], v[124:125]\n\t"
    "v_pk_mul_f32 v[32:33], v[32:33], v[128:129]\n\t"
    "v_pk_mul_f32 v[34:35], v[34:35], v[128:129]\n\t"
    "v_pk_mul_f32 v[36:37], v[36:37], v[128:129]\n\t"
    "v_pk_mul_f32 v[38:39], v[38:39], v[128:129]\n\t"
    "v_pk_mul_f32 v[40:41], v[40:41], v[128:129]\n\t"
    "ds_write_b128 v111, a[156:159] offset:19456\n\t"
    "v_pk_mul_f32 v[42:43], v[42:43], v[128:129]\n\t"
    "v_pk_mul_f32 v[44:45], v[44:45], v[128:129]\n\t"
    "v_pk_mul_f32 v[46:47], v[46:47], v[128:129]\n\t"
    "v_pk_mul_f32 v[48:49], v[48:49], v[128:129]\n\t"
    "v_pk_mul_f32 v[50:51], v[50:51], v[128:129]\n\t"
    "v_pk_mul_f32 v[52:53], v[52:53], v[128:129]\n\t"
    "v_pk_mul_f32 v[54:55], v[54:55], v[128:129]\n\t"
    "v_pk_mul_f32 v[56:57], v[56:57], v[128:129]\n\t"
    "v_pk_mul_f32 v[58:59], v[58:59], v[128:129]\n\t"
    "v_pk_mul_f32 v[60:61], v[60:61], v[128:129]\n\t"
    "v_pk_mul_f32 v[62:63], v[62:63], v[128:129]\n\t"
    "s_waitcnt lgkmcnt(0)\n\t"
    "s_barrier\n\t"
    "ds_read_b128 v[132:135], v112 offset:0\n\t"
    "ds_read_b128 v[136:139], v112 offset:1024\n\t"
    "ds_read_b128 v[140:143], v112 offset:2048\n\t"
    "ds_read_b128 v[144:147], v112 offset:3072\n\t"
    "ds_read_b128 v[148:151], v112 offset:16384\n\t"
    "ds_read_b128 v[152:155], v112 offset:17408\n\t"
    "ds_read_b128 v[156:159], v112 offset:18432\n\t"
    "ds_read_b128 v[160:163], v112 offset:19456\n\t"
    "ds_read_b128 v[164:167], v112 offset:32768\n\t"
    "ds_read_b128 v[168:171], v112 offset:33792\n\t"
    "ds_read_b128 v[172:175], v112 offset:34816\n\t"
    "ds_read_b128 v[176:179], v112 offset:35840\n\t"
    "ds_read_b128 v[180:183], v112 offset:49152\n\t"
    "ds_read_b128 v[184:187], v112 offset:50176\n\t"
    "ds_read_b128 v[188:191], v112 offset:51200\n\t"
    "ds_read_b128 v[192:195], v112 offset:52224\n\t"
    "s_waitcnt lgkmcnt(12)\n\t"
    "v_pk_fma_f32 v[0:1], v[132:133], v[126:127], v[0:1]\n\t"
    "v_pk_fma_f32 v[2:3], v[134:135], v[126:127], v[2:3]\n\t"
    "v_pk_fma_f32 v[4:5], v[136:137], v[126:127], v[4:5]\n\t"
    "v_pk_fma_f32 v[6:7], v[138:139], v[126:127], v[6:7]\n\t"
    "v_pk_fma_f32 v[8:9], v[140:141], v[126:127], v[8:9]\n\t"
    "v_pk_fma_f32 v[10:11], v[142:143], v[126:127], v[10:11]\n\t"
    "v_pk_fma_f32 v[12:13], v[144:145], v[126:127], v[12:13]\n\t"
    "v_pk_fma_f32 v[14:15], v[146:147], v[126:127], v[14:15]\n\t"
    "v_cvt_pk_bf16_f32 v72, v0, v1\n\t"
    "v_cvt_pk_bf16_f32 v73, v2, v3\n\t"
    "v_cvt_pk_bf16_f32 v74, v4, v5\n\t"
    "v_cvt_pk_bf16_f32 v75, v6, v7\n\t"
    "v_cvt_pk_bf16_f32 v76, v8, v9\n\t"
    "v_cvt_pk_bf16_f32 v77, v10, v11\n\t"
    "v_cvt_pk_bf16_f32 v78, v12, v13\n\t"
    "v_cvt_pk_bf16_f32 v79, v14, v15\n\t"
    "v_permlane32_swap_b32 v72, v74\n\t"
    "v_permlane32_swap_b32 v73, v75\n\t"
    "v_permlane32_swap_b32 v76, v78\n\t"
    "v_permlane32_swap_b32 v77, v79\n\t"
    "s_nop 0\n\t"
    "v_permlane16_swap_b32 v72, v76\n\t"
    "v_permlane16_swap_b32 v73, v77\n\t"
    "v_permlane16_swap_b32 v74, v78\n\t"
    "v_permlane16_swap_b32 v75, v79\n\t"
    "buffer_store_dwordx4 v[72:75], v117, s[52:55], 0 offen offset:0 sc1\n\t"
    "s_waitcnt lgkmcnt(8)\n\t"
    "v_pk_fma_f32 v[16:17], v[148:149], v[126:127], v[16:17]\n\t"
    "v_pk_fma_f32 v[18:19], v[150:151], v[126:127], v[18:19]\n\t"
    "v_pk_fma_f32 v[20:21], v[152:153], v[126:127], v[20:21]\n\t"
    "v_pk_fma_f32 v[22:23], v[154:155], v[126:127], v[22:23]\n\t"
    "v_pk_fma_f32 v[24:25], v[156:157], v[126:127], v[24:25]\n\t"
    "v_pk_fma_f32 v[26:27], v[158:159], v[126:127], v[26:27]\n\t"
    "v_pk_fma_f32 v[28:29], v[160:161], v[126:127], v[28:29]\n\t"
    "v_pk_fma_f32 v[30:31], v[162:163], v[126:127], v[30:31]\n\t"
    "v_cvt_pk_bf16_f32 v80, v16, v17\n\t"
    "v_cvt_pk_bf16_f32 v81, v18, v19\n\t"
    "v_cvt_pk_bf16_f32 v82, v20, v21\n\t"
    "v_cvt_pk_bf16_f32 v83, v22, v23\n\t"
    "v_cvt_pk_bf16_f32 v84, v24, v25\n\t"
    "v_cvt_pk_bf16_f32 v85, v26, v27\n\t"
    "v_cvt_pk_bf16_f32 v86, v28, v29\n\t"
    "v_cvt_pk_bf16_f32 v87, v30, v31\n\t"
    "buffer_store_dwordx4 v[76:79], v117, s[52:55], s81 offen offset:0 sc1\n\t"
    "v_permlane32_swap_b32 v80, v82\n\t"
    "v_permlane32_swap_b32 v81, v83\n\t"
    "v_permlane32_swap_b32 v84, v86\n\t"
    "v_permlane32_swap_b32 v85, v87\n\t"
    "s_nop 0\n\t"
    "v_permlane16_swap_b32 v80, v84\n\t"
    "v_permlane16_swap_b32 v81, v85\n\t"
    "v_permlane16_swap_b32 v82, v86\n\t"
    "v_permlane16_swap_b32 v83, v87\n\t"
    "buffer_store_dwordx4 v[80:83], v117, s[52:55], 0 offen offset:64 sc1\n\t"
    "ds_read_b128 v[196:199], v114 offset:0\n\t"
    "ds_read_b128 v[200:203], v114 offset:1024\n\t"
    "ds_read_b128 v[204:207], v115 offset:4096\n\t"
    "ds_read_b128 v[208:211], v115 offset:5120\n\t"
    "ds_read_b128 v[212:215], v114 offset:8192\n\t"
    "ds_read_b128 v[216:219], v114 offset:9216\n\t"
    "ds_read_b128 v[220:223], v115 offset:12288\n\t"
    "ds_read_b128 v[224:227], v115 offset:13312\n\t"
    "s_waitcnt lgkmcnt(12)\n\t"
    "v_pk_fma_f32 v[32:33], v[164:165], v[130:131], v[32:33]\n\t"
    "v_pk_fma_f32 v[34:35], v[166:167], v[130:131], v[34:35]\n\t"
    "v_pk_fma_f32 v[36:37], v[168:169], v[130:131], v[36:37]\n\t"
    "v_pk_fma_f32 v[38:39], v[170:171], v[130:131], v[38:39]\n\t"
    "v_pk_fma_f32 v[40:41], v[172:173], v[130:131], v[40:41]\n\t"
    "v_pk_fma_f32 v[42:43], v[174:175], v[130:131], v[42:43]\n\t"
    "v_pk_fma_f32 v[44:45], v[176:177], v[130:131], v[44:45]\n\t"
    "v_pk_fma_f32 v[46:47], v[178:179], v[130:131], v[46:47]\n\t"
    "v_cvt_pk_bf16_f32 v88, v32, v33\n\t"
    "v_cvt_pk_bf16_f32 v89, v34, v35\n\t"
    "v_cvt_pk_bf16_f32 v90, v36, v37\n\t"
    "v_cvt_pk_bf16_f32 v91, v38, v39\n\t"
    "v_cvt_pk_bf16_f32 v92, v40, v41\n\t"
    "v_cvt_pk_bf16_f32 v93, v42, v43\n\t"
    "v_cvt_pk_bf16_f32 v94, v44, v45\n\t"
    "v_cvt_pk_bf16_f32 v95, v46, v47\n\t"
    "buffer_store_dwordx4 v[84:87], v117, s[52:55], s81 offen offset:64 sc1\n\t"
    "v_permlane32_swap_b32 v88, v90\n\t"
    "v_permlane32_swap_b32 v89, v91\n\t"
    "v_permlane32_swap_b32 v92, v94\n\t"
    "v_permlane32_swap_b32 v93, v95\n\t"
    "s_nop 0\n\t"
    "v_permlane16_swap_b32 v88, v92\n\t"
    "v_permlane16_swap_b32 v89, v93\n\t"
    "v_permlane16_swap_b32 v90, v94\n\t"
    "v_permlane16_swap_b32 v91, v95\n\t"
    "buffer_store_dwordx4 v[88:91], v117, s[52:55], s82 offen offset:0 sc1\n\t"
    "s_waitcnt lgkmcnt(8)\n\t"
    "v_pk_fma_f32 v[48:49], v[180:181], v[130:131], v[48:49]\n\t"
    "v_pk_fma_f32 v[50:51], v[182:183], v[130:131], v[50:51]\n\t"
    "v_pk_fma_f32 v[52:53], v[184:185], v[130:131], v[52:53]\n\t"
    "v_pk_fma_f32 v[54:55], v[186:187], v[130:131], v[54:55]\n\t"
    "v_pk_fma_f32 v[56:57], v[188:189], v[130:131], v[56:57]\n\t"
    "v_pk_fma_f32 v[58:59], v[190:191], v[130:131], v[58:59]\n\t"
    "v_pk_fma_f32 v[60:61], v[192:193], v[130:131], v[60:61]\n\t"
    "v_pk_fma_f32 v[62:63], v[194:195], v[130:131], v[62:63]\n\t"
    "v_cvt_pk_bf16_f32 v72, v48, v49\n\t"
    "v_cvt_pk_bf16_f32 v73, v50, v51\n\t"
    "v_cvt_pk_bf16_f32 v74, v52, v53\n\t"
    "v_cvt_pk_bf16_f32 v75, v54, v55\n\t"
    "v_cvt_pk_bf16_f32 v76, v56, v57\n\t"
    "v_cvt_pk_bf16_f32 v77, v58, v59\n\t"
    "v_cvt_pk_bf16_f32 v78, v60, v61\n\t"
    "v_cvt_pk_bf16_f32 v79, v62, v63\n\t"
    "buffer_store_dwordx4 v[92:95], v117, s[52:55], s83 offen offset:0 sc1\n\t"
    "v_permlane32_swap_b32 v72, v74\n\t"
    "v_permlane32_swap_b32 v73, v75\n\t"
    "v_permlane32_swap_b32 v76, v78\n\t"
    "v_permlane32_swap_b32 v77, v79\n\t"
    "s_nop 0\n\t"
    "v_permlane16_swap_b32 v72, v76\n\t"
    "v_permlane16_swap_b32 v73, v77\n\t"
    "v_permlane16_swap_b32 v74, v78\n\t"
    "v_permlane16_swap_b32 v75, v79\n\t"
    "buffer_store_dwordx4 v[72:75], v117, s[52:55], s82 offen offset:64 sc1\n\t"
    "s_waitcnt lgkmcnt(0)\n\t"
    "v_pk_mul_f32 v[196:197], v[196:197], v[64:65]\n\t"
    "v_pk_mul_f32 v[198:199], v[198:199], v[64:65]\n\t"
    "v_pk_mul_f32 v[200:201], v[200:201], v[64:65]\n\t"
    "v_pk_mul_f32 v[202:203], v[202:203], v[64:65]\n\t"
    "v_pk_fma_f32 v[196:197], v[204:205], v[66:67], v[196:197]\n\t"
    "v_pk_fma_f32 v[198:199], v[206:207], v[66:67], v[198:199]\n\t"
    "v_pk_fma_f32 v[200:201], v[208:209], v[66:67], v[200:201]\n\t"
    "v_pk_fma_f32 v[202:203], v[210:211], v[66:67], v[202:203]\n\t"
    "v_pk_fma_f32 v[196:197], v[212:213], v[68:69], v[196:197]\n\t"
    "v_pk_fma_f32 v[198:199], v[214:215], v[68:69], v[198:199]\n\t"
    "v_pk_fma_f32 v[200:201], v[216:217], v[68:69], v[200:201]\n\t"
    "v_pk_fma_f32 v[202:203], v[218:219], v[68:69], v[202:203]\n\t"
    "v_pk_fma_f32 v[196:197], v[220:221], v[70:71], v[196:197]\n\t"
    "v_pk_fma_f32 v[198:199], v[222:223], v[70:71], v[198:199]\n\t"
    "v_pk_fma_f32 v[200:201], v[224:225], v[70:71], v[200:201]\n\t"
    "v_pk_fma_f32 v[202:203], v[226:227], v[70:71], v[202:203]\n\t"
    "buffer_store_dwordx4 v[76:79], v117, s[52:55], s83 offen offset:64 sc1\n\t"
    "v_cvt_pk_bf16_f32 v96, v196, v197\n\t"
    "v_cvt_pk_bf16_f32 v97, v198, v199\n\t"
    "v_cvt_pk_bf16_f32 v98, v200, v201\n\t"
    "v_cvt_pk_bf16_f32 v99, v202, v203\n\t"
    "s_nop 1\n\t"
    "v_permlane16_swap_b32 v96, v98\n\t"
    "v_permlane16_swap_b32 v97, v99\n\t"
    "buffer_store_dwordx4 v[96:99], v118, s[52:55], 0 offen offset:0 sc1\n\t"
    "s_endpgm\n\t"
    "L_rare_c1e_%=:\n\t"
    "s_nop 15\n\t"
    "v_max3_f32 v241, v16, v17, v18\n\t"
    "v_max3_f32 v241, v241, v19, v20\n\t"
    "v_max3_f32 v241, v241, v21, v22\n\t"
    "v_max3_f32 v241, v241, v23, v24\n\t"
    "v_max3_f32 v241, v241, v25, v26\n\t"
    "v_max3_f32 v241, v241, v27, v28\n\t"
    "v_max3_f32 v241, v241, v29, v30\n\t"
    "v_max_f32 v241, v241, v31\n\t"
    "v_mov_b32 v242, v241\n\t"
    "s_nop 1\n\t"
    "v_permlane32_swap_b32 v241, v242\n\t"
    "v_max_f32 v241, v241, v242\n\t"
    "v_ceil_f32 v241, v241\n\t"
    "v_max_f32 v241, 0, v241\n\t"
    "v_sub_f32 v243, 0, v241\n\t"
    "v_exp_f32 v243, v243\n\t"
    "v_sub_f32 v244, v48, v241\n\t"
    "v_mov_b32 v48, v244\n\t"
    "v_mov_b32 v49, v244\n\t"
    "v_mov_b32 v50, v244\n\t"
    "v_mov_b32 v51, v244\n\t"
    "v_mov_b32 v52, v244\n\t"
    "v_mov_b32 v53, v244\n\t"
    "v_mov_b32 v54, v244\n\t"
    "v_mov_b32 v55, v244\n\t"
    "v_mov_b32 v56, v244\n\t"
    "v_mov_b32 v57, v244\n\t"
    "v_mov_b32 v58, v244\n\t"
    "v_mov_b32 v59, v244\n\t"
    "v_mov_b32 v60, v244\n\t"
    "v_mov_b32 v61, v244\n\t"
    "v_mov_b32 v62, v244\n\t"
    "v_mov_b32 v63, v244\n\t"
    "v_mul_f32 v228, v228, v243\n\t"
    "v_accvgpr_read_b32 v244, a16\n\t"
    "v_mul_f32 v244, v244, v243\n\t"
    "v_accvgpr_write_b32 a16, v244\n\t"
    "v_accvgpr_read_b32 v245, a17\n\t"
    "v_mul_f32 v245, v245, v243\n\t"
    "v_accvgpr_write_b32 a17, v245\n\t"
    "v_accvgpr_read_b32 v244, a18\n\t"
    "v_mul_f32 v244, v244, v243\n\t"
    "v_accvgpr_write_b32 a18, v244\n\t"
    "v_accvgpr_read_b32 v245, a19\n\t"
    "v_mul_f32 v245, v245, v243\n\t"
    "v_accvgpr_write_b32 a19, v245\n\t"
    "v_accvgpr_read_b32 v244, a20\n\t"
    "v_mul_f32 v244, v244, v243\n\t"
    "v_accvgpr_write_b32 a20, v244\n\t"
    "v_accvgpr_read_b32 v245, a21\n\t"
    "v_mul_f32 v245, v245, v243\n\t"
    "v_accvgpr_write_b32 a21, v245\n\t"
    "v_accvgpr_read_b32 v244, a22\n\t"
    "v_mul_f32 v244, v244, v243\n\t"
    "v_accvgpr_write_b32 a22, v244\n\t"
    "v_accvgpr_read_b32 v245, a23\n\t"
    "v_mul_f32 v245, v245, v243\n\t"
    "v_accvgpr_write_b32 a23, v245\n\t"
    "v_accvgpr_read_b32 v244, a24\n\t"
    "v_mul_f32 v244, v244, v243\n\t"
    "v_accvgpr_write_b32 a24, v244\n\t"
    "v_accvgpr_read_b32 v245, a25\n\t"
    "v_mul_f32 v245, v245, v243\n\t"
    "v_accvgpr_write_b32 a25, v245\n\t"
    "v_accvgpr_read_b32 v244, a26\n\t"
    "v_mul_f32 v244, v244, v243\n\t"
    "v_accvgpr_write_b32 a26, v244\n\t"
    "v_accvgpr_read_b32 v245, a27\n\t"
    "v_mul_f32 v245, v245, v243\n\t"
    "v_accvgpr_write_b32 a27, v245\n\t"
    "v_accvgpr_read_b32 v244, a28\n\t"
    "v_mul_f32 v244, v244, v243\n\t"
    "v_accvgpr_write_b32 a28, v244\n\t"
    "v_accvgpr_read_b32 v245, a29\n\t"
    "v_mul_f32 v245, v245, v243\n\t"
    "v_accvgpr_write_b32 a29, v245\n\t"
    "v_accvgpr_read_b32 v244, a30\n\t"
    "v_mul_f32 v244, v244, v243\n\t"
    "v_accvgpr_write_b32 a30, v244\n\t"
    "v_accvgpr_read_b32 v245, a31\n\t"
    "v_mul_f32 v245, v245, v243\n\t"
    "v_accvgpr_write_b32 a31, v245\n\t"
    "v_accvgpr_read_b32 v244, a48\n\t"
    "v_mul_f32 v244, v244, v243\n\t"
    "v_accvgpr_write_b32 a48, v244\n\t"
    "v_accvgpr_read_b32 v245, a49\n\t"
    "v_mul_f32 v245, v245, v243\n\t"
    "v_accvgpr_write_b32 a49, v245\n\t"
    "v_accvgpr_read_b32 v244, a50\n\t"
    "v_mul_f32 v244, v244, v243\n\t"
    "v_accvgpr_write_b32 a50, v244\n\t"
    "v_accvgpr_read_b32 v245, a51\n\t"
    "v_mul_f32 v245, v245, v243\n\t"
    "v_accvgpr_write_b32 a51, v245\n\t"
    "v_accvgpr_read_b32 v244, a52\n\t"
    "v_mul_f32 v244, v244, v243\n\t"
    "v_accvgpr_write_b32 a52, v244\n\t"
    "v_accvgpr_read_b32 v245, a53\n\t"
    "v_mul_f32 v245, v245, v243\n\t"
    "v_accvgpr_write_b32 a53, v245\n\t"
    "v_accvgpr_read_b32 v244, a54\n\t"
    "v_mul_f32 v244, v244, v243\n\t"
    "v_accvgpr_write_b32 a54, v244\n\t"
    "v_accvgpr_read_b32 v245, a55\n\t"
    "v_mul_f32 v245, v245, v243\n\t"
    "v_accvgpr_write_b32 a55, v245\n\t"
    "v_accvgpr_read_b32 v244, a56\n\t"
    "v_mul_f32 v244, v244, v243\n\t"
    "v_accvgpr_write_b32 a56, v244\n\t"
    "v_accvgpr_read_b32 v245, a57\n\t"
    "v_mul_f32 v245, v245, v243\n\t"
    "v_accvgpr_write_b32 a57, v245\n\t"
    "v_accvgpr_read_b32 v244, a58\n\t"
    "v_mul_f32 v244, v244, v243\n\t"
    "v_accvgpr_write_b32 a58, v244\n\t"
    "v_accvgpr_read_b32 v245, a59\n\t"
    "v_mul_f32 v245, v245, v243\n\t"
    "v_accvgpr_write_b32 a59, v245\n\t"
    "v_accvgpr_read_b32 v244, a60\n\t"
    "v_mul_f32 v244, v244, v243\n\t"
    "v_accvgpr_write_b32 a60, v244\n\t"
    "v_accvgpr_read_b32 v245, a61\n\t"
    "v_mul_f32 v245, v245, v243\n\t"
    "v_accvgpr_write_b32 a61, v245\n\t"
    "v_accvgpr_read_b32 v244, a62\n\t"
    "v_mul_f32 v244, v244, v243\n\t"
    "v_accvgpr_write_b32 a62, v244\n\t"
    "v_accvgpr_read_b32 v245, a63\n\t"
    "v_mul_f32 v245, v245, v243\n\t"
    "v_accvgpr_write_b32 a63, v245\n\t"
    "v_accvgpr_read_b32 v244, a80\n\t"
    "v_mul_f32 v244, v244, v243\n\t"
    "v_accvgpr_write_b32 a80, v244\n\t"
    "v_accvgpr_read_b32 v245, a81\n\t"
    "v_mul_f32 v245, v245, v243\n\t"
    "v_accvgpr_write_b32 a81, v245\n\t"
    "v_accvgpr_read_b32 v244, a82\n\t"
    "v_mul_f32 v244, v244, v243\n\t"
    "v_accvgpr_write_b32 a82, v244\n\t"
    "v_accvgpr_read_b32 v245, a83\n\t"
    "v_mul_f32 v245, v245, v243\n\t"
    "v_accvgpr_write_b32 a83, v245\n\t"
    "v_accvgpr_read_b32 v244, a84\n\t"
    "v_mul_f32 v244, v244, v243\n\t"
    "v_accvgpr_write_b32 a84, v244\n\t"
    "v_accvgpr_read_b32 v245, a85\n\t"
    "v_mul_f32 v245, v245, v243\n\t"
    "v_accvgpr_write_b32 a85, v245\n\t"
    "v_accvgpr_read_b32 v244, a86\n\t"
    "v_mul_f32 v244, v244, v243\n\t"
    "v_accvgpr_write_b32 a86, v244\n\t"
    "v_accvgpr_read_b32 v245, a87\n\t"
    "v_mul_f32 v245, v245, v243\n\t"
    "v_accvgpr_write_b32 a87, v245\n\t"
    "v_accvgpr_read_b32 v244, a88\n\t"
    "v_mul_f32 v244, v244, v243\n\t"
    "v_accvgpr_write_b32 a88, v244\n\t"
    "v_accvgpr_read_b32 v245, a89\n\t"
    "v_mul_f32 v245, v245, v243\n\t"
    "v_accvgpr_write_b32 a89, v245\n\t"
    "v_accvgpr_read_b32 v244, a90\n\t"
    "v_mul_f32 v244, v244, v243\n\t"
    "v_accvgpr_write_b32 a90, v244\n\t"
    "v_accvgpr_read_b32 v245, a91\n\t"
    "v_mul_f32 v245, v245, v243\n\t"
    "v_accvgpr_write_b32 a91, v245\n\t"
    "v_accvgpr_read_b32 v244, a92\n\t"
    "v_mul_f32 v244, v244, v243\n\t"
    "v_accvgpr_write_b32 a92, v244\n\t"
    "v_accvgpr_read_b32 v245, a93\n\t"
    "v_mul_f32 v245, v245, v243\n\t"
    "v_accvgpr_write_b32 a93, v245\n\t"
    "v_accvgpr_read_b32 v244, a94\n\t"
    "v_mul_f32 v244, v244, v243\n\t"
    "v_accvgpr_write_b32 a94, v244\n\t"
    "v_accvgpr_read_b32 v245, a95\n\t"
    "v_mul_f32 v245, v245, v243\n\t"
    "v_accvgpr_write_b32 a95, v245\n\t"
    "v_accvgpr_read_b32 v244, a112\n\t"
    "v_mul_f32 v244, v244, v243\n\t"
    "v_accvgpr_write_b32 a112, v244\n\t"
    "v_accvgpr_read_b32 v245, a113\n\t"
    "v_mul_f32 v245, v245, v243\n\t"
    "v_accvgpr_write_b32 a113, v245\n\t"
    "v_accvgpr_read_b32 v244, a114\n\t"
    "v_mul_f32 v244, v244, v243\n\t"
    "v_accvgpr_write_b32 a114, v244\n\t"
    "v_accvgpr_read_b32 v245, a115\n\t"
    "v_mul_f32 v245, v245, v243\n\t"
    "v_accvgpr_write_b32 a115, v245\n\t"
    "v_accvgpr_read_b32 v244, a116\n\t"
    "v_mul_f32 v244, v244, v243\n\t"
    "v_accvgpr_write_b32 a116, v244\n\t"
    "v_accvgpr_read_b32 v245, a117\n\t"
    "v_mul_f32 v245, v245, v243\n\t"
    "v_accvgpr_write_b32 a117, v245\n\t"
    "v_accvgpr_read_b32 v244, a118\n\t"
    "v_mul_f32 v244, v244, v243\n\t"
    "v_accvgpr_write_b32 a118, v244\n\t"
    "v_accvgpr_read_b32 v245, a119\n\t"
    "v_mul_f32 v245, v245, v243\n\t"
    "v_accvgpr_write_b32 a119, v245\n\t"
    "v_accvgpr_read_b32 v244, a120\n\t"
    "v_mul_f32 v244, v244, v243\n\t"
    "v_accvgpr_write_b32 a120, v244\n\t"
    "v_accvgpr_read_b32 v245, a121\n\t"
    "v_mul_f32 v245, v245, v243\n\t"
    "v_accvgpr_write_b32 a121, v245\n\t"
    "v_accvgpr_read_b32 v244, a122\n\t"
    "v_mul_f32 v244, v244, v243\n\t"
    "v_accvgpr_write_b32 a122, v244\n\t"
    "v_accvgpr_read_b32 v245, a123\n\t"
    "v_mul_f32 v245, v245, v243\n\t"
    "v_accvgpr_write_b32 a123, v245\n\t"
    "v_accvgpr_read_b32 v244, a124\n\t"
    "v_mul_f32 v244, v244, v243\n\t"
    "v_accvgpr_write_b32 a124, v244\n\t"
    "v_accvgpr_read_b32 v245, a125\n\t"
    "v_mul_f32 v245, v245, v243\n\t"
    "v_accvgpr_write_b32 a125, v245\n\t"
    "v_accvgpr_read_b32 v244, a126\n\t"
    "v_mul_f32 v244, v244, v243\n\t"
    "v_accvgpr_write_b32 a126, v244\n\t"
    "v_accvgpr_read_b32 v245, a127\n\t"
    "v_mul_f32 v245, v245, v243\n\t"
    "v_accvgpr_write_b32 a127, v245\n\t"
    "v_sub_f32 v244, v16, v241\n\t"
    "v_sub_f32 v245, v17, v241\n\t"
    "v_exp_f32 v244, v244\n\t"
    "v_exp_f32 v245, v245\n\t"
    "s_nop 0\n\t"
    "v_add_f32 v225, v244, v245\n\t"
    "v_cvt_pk_bf16_f32 v88, v244, v245\n\t"
    "v_sub_f32 v244, v18, v241\n\t"
    "v_sub_f32 v245, v19, v241\n\t"
    "v_exp_f32 v244, v244\n\t"
    "v_exp_f32 v245, v245\n\t"
    "s_nop 0\n\t"
    "v_add_f32 v225, v225, v244\n\t"
    "v_add_f32 v225, v225, v245\n\t"
    "v_cvt_pk_bf16_f32 v89, v244, v245\n\t"
    "v_sub_f32 v244, v20, v241\n\t"
    "v_sub_f32 v245, v21, v241\n\t"
    "v_exp_f32 v244, v244\n\t"
    "v_exp_f32 v245, v245\n\t"
    "s_nop 0\n\t"
    "v_add_f32 v225, v225, v244\n\t"
    "v_add_f32 v225, v225, v245\n\t"
    "v_cvt_pk_bf16_f32 v90, v244, v245\n\t"
    "v_sub_f32 v244, v22, v241\n\t"
    "v_sub_f32 v245, v23, v241\n\t"
    "v_exp_f32 v244, v244\n\t"
    "v_exp_f32 v245, v245\n\t"
    "s_nop 0\n\t"
    "v_add_f32 v225, v225, v244\n\t"
    "v_add_f32 v225, v225, v245\n\t"
    "v_cvt_pk_bf16_f32 v91, v244, v245\n\t"
    "v_sub_f32 v244, v24, v241\n\t"
    "v_sub_f32 v245, v25, v241\n\t"
    "v_exp_f32 v244, v244\n\t"
    "v_exp_f32 v245, v245\n\t"
    "s_nop 0\n\t"
    "v_add_f32 v225, v225, v244\n\t"
    "v_add_f32 v225, v225, v245\n\t"
    "v_cvt_pk_bf16_f32 v92, v244, v245\n\t"
    "v_sub_f32 v244, v26, v241\n\t"
    "v_sub_f32 v245, v27, v241\n\t"
    "v_exp_f32 v244, v244\n\t"
    "v_exp_f32 v245, v245\n\t"
    "s_nop 0\n\t"
    "v_add_f32 v225, v225, v244\n\t"
    "v_add_f32 v225, v225, v245\n\t"
    "v_cvt_pk_bf16_f32 v93, v244, v245\n\t"
    "v_sub_f32 v244, v28, v241\n\t"
    "v_sub_f32 v245, v29, v241\n\t"
    "v_exp_f32 v244, v244\n\t"
    "v_exp_f32 v245, v245\n\t"
    "s_nop 0\n\t"
    "v_add_f32 v225, v225, v244\n\t"
    "v_add_f32 v225, v225, v245\n\t"
    "v_cvt_pk_bf16_f32 v94, v244, v245\n\t"
    "v_sub_f32 v244, v30, v241\n\t"
    "v_sub_f32 v245, v31, v241\n\t"
    "v_exp_f32 v244, v244\n\t"
    "v_exp_f32 v245, v245\n\t"
    "s_nop 0\n\t"
    "v_add_f32 v225, v225, v244\n\t"
    "v_add_f32 v225, v225, v245\n\t"
    "v_cvt_pk_bf16_f32 v95, v244, v245\n\t"
    "s_nop 1\n\t"
    "s_branch L_ret_c1e_%=\n\t"
    "L_rare_c0e_%=:\n\t"
    "s_nop 15\n\t"
    "v_max3_f32 v241, v0, v1, v2\n\t"
    "v_max3_f32 v241, v241, v3, v4\n\t"
    "v_max3_f32 v241, v241, v5, v6\n\t"
    "v_max3_f32 v241, v241, v7, v8\n\t"
    "v_max3_f32 v241, v241, v9, v10\n\t"
    "v_max3_f32 v241, v241, v11, v12\n\t"
    "v_max3_f32 v241, v241, v13, v14\n\t"
    "v_max_f32 v241, v241, v15\n\t"
    "v_mov_b32 v242, v241\n\t"
    "s_nop 1\n\t"
    "v_permlane32_swap_b32 v241, v242\n\t"
    "v_max_f32 v241, v241, v242\n\t"
    "v_ceil_f32 v241, v241\n\t"
    "v_max_f32 v241, 0, v241\n\t"
    "v_sub_f32 v243, 0, v241\n\t"
    "v_exp_f32 v243, v243\n\t"
    "v_sub_f32 v244, v32, v241\n\t"
    "v_mov_b32 v32, v244\n\t"
    "v_mov_b32 v33, v244\n\t"
    "v_mov_b32 v34, v244\n\t"
    "v_mov_b32 v35, v244\n\t"
    "v_mov_b32 v36, v244\n\t"
    "v_mov_b32 v37, v244\n\t"
    "v_mov_b32 v38, v244\n\t"
    "v_mov_b32 v39, v244\n\t"
    "v_mov_b32 v40, v244\n\t"
    "v_mov_b32 v41, v244\n\t"
    "v_mov_b32 v42, v244\n\t"
    "v_mov_b32 v43, v244\n\t"
    "v_mov_b32 v44, v244\n\t"
    "v_mov_b32 v45, v244\n\t"
    "v_mov_b32 v46, v244\n\t"
    "v_mov_b32 v47, v244\n\t"
    "v_mul_f32 v227, v227, v243\n\t"
    "v_accvgpr_read_b32 v244, a0\n\t"
    "v_mul_f32 v244, v244, v243\n\t"
    "v_accvgpr_write_b32 a0, v244\n\t"
    "v_accvgpr_read_b32 v245, a1\n\t"
    "v_mul_f32 v245, v245, v243\n\t"
    "v_accvgpr_write_b32 a1, v245\n\t"
    "v_accvgpr_read_b32 v244, a2\n\t"
    "v_mul_f32 v244, v244, v243\n\t"
    "v_accvgpr_write_b32 a2, v244\n\t"
    "v_accvgpr_read_b32 v245, a3\n\t"
    "v_mul_f32 v245, v245, v243\n\t"
    "v_accvgpr_write_b32 a3, v245\n\t"
    "v_accvgpr_read_b32 v244, a4\n\t"
    "v_mul_f32 v244, v244, v243\n\t"
    "v_accvgpr_write_b32 a4, v244\n\t"
    "v_accvgpr_read_b32 v245, a5\n\t"
    "v_mul_f32 v245, v245, v243\n\t"
    "v_accvgpr_write_b32 a5, v245\n\t"
    "v_accvgpr_read_b32 v244, a6\n\t"
    "v_mul_f32 v244, v244, v243\n\t"
    "v_accvgpr_write_b32 a6, v244\n\t"
    "v_accvgpr_read_b32 v245, a7\n\t"
    "v_mul_f32 v245, v245, v243\n\t"
    "v_accvgpr_write_b32 a7, v245\n\t"
    "v_accvgpr_read_b32 v244, a8\n\t"
    "v_mul_f32 v244, v244, v243\n\t"
    "v_accvgpr_write_b32 a8, v244\n\t"
    "v_accvgpr_read_b32 v245, a9\n\t"
    "v_mul_f32 v245, v245, v243\n\t"
    "v_accvgpr_write_b32 a9, v245\n\t"
    "v_accvgpr_read_b32 v244, a10\n\t"
    "v_mul_f32 v244, v244, v243\n\t"
    "v_accvgpr_write_b32 a10, v244\n\t"
    "v_accvgpr_read_b32 v245, a11\n\t"
    "v_mul_f32 v245, v245, v243\n\t"
    "v_accvgpr_write_b32 a11, v245\n\t"
    "v_accvgpr_read_b32 v244, a12\n\t"
    "v_mul_f32 v244, v244, v243\n\t"
    "v_accvgpr_write_b32 a12, v244\n\t"
    "v_accvgpr_read_b32 v245, a13\n\t"
    "v_mul_f32 v245, v245, v243\n\t"
    "v_accvgpr_write_b32 a13, v245\n\t"
    "v_accvgpr_read_b32 v244, a14\n\t"
    "v_mul_f32 v244, v244, v243\n\t"
    "v_accvgpr_write_b32 a14, v244\n\t"
    "v_accvgpr_read_b32 v245, a15\n\t"
    "v_mul_f32 v245, v245, v243\n\t"
    "v_accvgpr_write_b32 a15, v245\n\t"
    "v_accvgpr_read_b32 v244, a32\n\t"
    "v_mul_f32 v244, v244, v243\n\t"
    "v_accvgpr_write_b32 a32, v244\n\t"
    "v_accvgpr_read_b32 v245, a33\n\t"
    "v_mul_f32 v245, v245, v243\n\t"
    "v_accvgpr_write_b32 a33, v245\n\t"
    "v_accvgpr_read_b32 v244, a34\n\t"
    "v_mul_f32 v244, v244, v243\n\t"
    "v_accvgpr_write_b32 a34, v244\n\t"
    "v_accvgpr_read_b32 v245, a35\n\t"
    "v_mul_f32 v245, v245, v243\n\t"
    "v_accvgpr_write_b32 a35, v245\n\t"
    "v_accvgpr_read_b32 v244, a36\n\t"
    "v_mul_f32 v244, v244, v243\n\t"
    "v_accvgpr_write_b32 a36, v244\n\t"
    "v_accvgpr_read_b32 v245, a37\n\t"
    "v_mul_f32 v245, v245, v243\n\t"
    "v_accvgpr_write_b32 a37, v245\n\t"
    "v_accvgpr_read_b32 v244, a38\n\t"
    "v_mul_f32 v244, v244, v243\n\t"
    "v_accvgpr_write_b32 a38, v244\n\t"
    "v_accvgpr_read_b32 v245, a39\n\t"
    "v_mul_f32 v245, v245, v243\n\t"
    "v_accvgpr_write_b32 a39, v245\n\t"
    "v_accvgpr_read_b32 v244, a40\n\t"
    "v_mul_f32 v244, v244, v243\n\t"
    "v_accvgpr_write_b32 a40, v244\n\t"
    "v_accvgpr_read_b32 v245, a41\n\t"
    "v_mul_f32 v245, v245, v243\n\t"
    "v_accvgpr_write_b32 a41, v245\n\t"
    "v_accvgpr_read_b32 v244, a42\n\t"
    "v_mul_f32 v244, v244, v243\n\t"
    "v_accvgpr_write_b32 a42, v244\n\t"
    "v_accvgpr_read_b32 v245, a43\n\t"
    "v_mul_f32 v245, v245, v243\n\t"
    "v_accvgpr_write_b32 a43, v245\n\t"
    "v_accvgpr_read_b32 v244, a44\n\t"
    "v_mul_f32 v244, v244, v243\n\t"
    "v_accvgpr_write_b32 a44, v244\n\t"
    "v_accvgpr_read_b32 v245, a45\n\t"
    "v_mul_f32 v245, v245, v243\n\t"
    "v_accvgpr_write_b32 a45, v245\n\t"
    "v_accvgpr_read_b32 v244, a46\n\t"
    "v_mul_f32 v244, v244, v243\n\t"
    "v_accvgpr_write_b32 a46, v244\n\t"
    "v_accvgpr_read_b32 v245, a47\n\t"
    "v_mul_f32 v245, v245, v243\n\t"
    "v_accvgpr_write_b32 a47, v245\n\t"
    "v_accvgpr_read_b32 v244, a64\n\t"
    "v_mul_f32 v244, v244, v243\n\t"
    "v_accvgpr_write_b32 a64, v244\n\t"
    "v_accvgpr_read_b32 v245, a65\n\t"
    "v_mul_f32 v245, v245, v243\n\t"
    "v_accvgpr_write_b32 a65, v245\n\t"
    "v_accvgpr_read_b32 v244, a66\n\t"
    "v_mul_f32 v244, v244, v243\n\t"
    "v_accvgpr_write_b32 a66, v244\n\t"
    "v_accvgpr_read_b32 v245, a67\n\t"
    "v_mul_f32 v245, v245, v243\n\t"
    "v_accvgpr_write_b32 a67, v245\n\t"
    "v_accvgpr_read_b32 v244, a68\n\t"
    "v_mul_f32 v244, v244, v243\n\t"
    "v_accvgpr_write_b32 a68, v244\n\t"
    "v_accvgpr_read_b32 v245, a69\n\t"
    "v_mul_f32 v245, v245, v243\n\t"
    "v_accvgpr_write_b32 a69, v245\n\t"
    "v_accvgpr_read_b32 v244, a70\n\t"
    "v_mul_f32 v244, v244, v243\n\t"
    "v_accvgpr_write_b32 a70, v244\n\t"
    "v_accvgpr_read_b32 v245, a71\n\t"
    "v_mul_f32 v245, v245, v243\n\t"
    "v_accvgpr_write_b32 a71, v245\n\t"
    "v_accvgpr_read_b32 v244, a72\n\t"
    "v_mul_f32 v244, v244, v243\n\t"
    "v_accvgpr_write_b32 a72, v244\n\t"
    "v_accvgpr_read_b32 v245, a73\n\t"
    "v_mul_f32 v245, v245, v243\n\t"
    "v_accvgpr_write_b32 a73, v245\n\t"
    "v_accvgpr_read_b32 v244, a74\n\t"
    "v_mul_f32 v244, v244, v243\n\t"
    "v_accvgpr_write_b32 a74, v244\n\t"
    "v_accvgpr_read_b32 v245, a75\n\t"
    "v_mul_f32 v245, v245, v243\n\t"
    "v_accvgpr_write_b32 a75, v245\n\t"
    "v_accvgpr_read_b32 v244, a76\n\t"
    "v_mul_f32 v244, v244, v243\n\t"
    "v_accvgpr_write_b32 a76, v244\n\t"
    "v_accvgpr_read_b32 v245, a77\n\t"
    "v_mul_f32 v245, v245, v243\n\t"
    "v_accvgpr_write_b32 a77, v245\n\t"
    "v_accvgpr_read_b32 v244, a78\n\t"
    "v_mul_f32 v244, v244, v243\n\t"
    "v_accvgpr_write_b32 a78, v244\n\t"
    "v_accvgpr_read_b32 v245, a79\n\t"
    "v_mul_f32 v245, v245, v243\n\t"
    "v_accvgpr_write_b32 a79, v245\n\t"
    "v_accvgpr_read_b32 v244, a96\n\t"
    "v_mul_f32 v244, v244, v243\n\t"
    "v_accvgpr_write_b32 a96, v244\n\t"
    "v_accvgpr_read_b32 v245, a97\n\t"
    "v_mul_f32 v245, v245, v243\n\t"
    "v_accvgpr_write_b32 a97, v245\n\t"
    "v_accvgpr_read_b32 v244, a98\n\t"
    "v_mul_f32 v244, v244, v243\n\t"
    "v_accvgpr_write_b32 a98, v244\n\t"
    "v_accvgpr_read_b32 v245, a99\n\t"
    "v_mul_f32 v245, v245, v243\n\t"
    "v_accvgpr_write_b32 a99, v245\n\t"
    "v_accvgpr_read_b32 v244, a100\n\t"
    "v_mul_f32 v244, v244, v243\n\t"
    "v_accvgpr_write_b32 a100, v244\n\t"
    "v_accvgpr_read_b32 v245, a101\n\t"
    "v_mul_f32 v245, v245, v243\n\t"
    "v_accvgpr_write_b32 a101, v245\n\t"
    "v_accvgpr_read_b32 v244, a102\n\t"
    "v_mul_f32 v244, v244, v243\n\t"
    "v_accvgpr_write_b32 a102, v244\n\t"
    "v_accvgpr_read_b32 v245, a103\n\t"
    "v_mul_f32 v245, v245, v243\n\t"
    "v_accvgpr_write_b32 a103, v245\n\t"
    "v_accvgpr_read_b32 v244, a104\n\t"
    "v_mul_f32 v244, v244, v243\n\t"
    "v_accvgpr_write_b32 a104, v244\n\t"
    "v_accvgpr_read_b32 v245, a105\n\t"
    "v_mul_f32 v245, v245, v243\n\t"
    "v_accvgpr_write_b32 a105, v245\n\t"
    "v_accvgpr_read_b32 v244, a106\n\t"
    "v_mul_f32 v244, v244, v243\n\t"
    "v_accvgpr_write_b32 a106, v244\n\t"
    "v_accvgpr_read_b32 v245, a107\n\t"
    "v_mul_f32 v245, v245, v243\n\t"
    "v_accvgpr_write_b32 a107, v245\n\t"
    "v_accvgpr_read_b32 v244, a108\n\t"
    "v_mul_f32 v244, v244, v243\n\t"
    "v_accvgpr_write_b32 a108, v244\n\t"
    "v_accvgpr_read_b32 v245, a109\n\t"
    "v_mul_f32 v245, v245, v243\n\t"
    "v_accvgpr_write_b32 a109, v245\n\t"
    "v_accvgpr_read_b32 v244, a110\n\t"
    "v_mul_f32 v244, v244, v243\n\t"
    "v_accvgpr_write_b32 a110, v244\n\t"
    "v_accvgpr_read_b32 v245, a111\n\t"
    "v_mul_f32 v245, v245, v243\n\t"
    "v_accvgpr_write_b32 a111, v245\n\t"
    "v_sub_f32 v244, v0, v241\n\t"
    "v_sub_f32 v245, v1, v241\n\t"
    "v_exp_f32 v244, v244\n\t"
    "v_exp_f32 v245, v245\n\t"
    "s_nop 0\n\t"
    "v_add_f32 v224, v244, v245\n\t"
    "v_cvt_pk_bf16_f32 v72, v244, v245\n\t"
    "v_sub_f32 v244, v2, v241\n\t"
    "v_sub_f32 v245, v3, v241\n\t"
    "v_exp_f32 v244, v244\n\t"
    "v_exp_f32 v245, v245\n\t"
    "s_nop 0\n\t"
    "v_add_f32 v224, v224, v244\n\t"
    "v_add_f32 v224, v224, v245\n\t"
    "v_cvt_pk_bf16_f32 v73, v244, v245\n\t"
    "v_sub_f32 v244, v4, v241\n\t"
    "v_sub_f32 v245, v5, v241\n\t"
    "v_exp_f32 v244, v244\n\t"
    "v_exp_f32 v245, v245\n\t"
    "s_nop 0\n\t"
    "v_add_f32 v224, v224, v244\n\t"
    "v_add_f32 v224, v224, v245\n\t"
    "v_cvt_pk_bf16_f32 v74, v244, v245\n\t"
    "v_sub_f32 v244, v6, v241\n\t"
    "v_sub_f32 v245, v7, v241\n\t"
    "v_exp_f32 v244, v244\n\t"
    "v_exp_f32 v245, v245\n\t"
    "s_nop 0\n\t"
    "v_add_f32 v224, v224, v244\n\t"
    "v_add_f32 v224, v224, v245\n\t"
    "v_cvt_pk_bf16_f32 v75, v244, v245\n\t"
    "v_sub_f32 v244, v8, v241\n\t"
    "v_sub_f32 v245, v9, v241\n\t"
    "v_exp_f32 v244, v244\n\t"
    "v_exp_f32 v245, v245\n\t"
    "s_nop 0\n\t"
    "v_add_f32 v224, v224, v244\n\t"
    "v_add_f32 v224, v224, v245\n\t"
    "v_cvt_pk_bf16_f32 v76, v244, v245\n\t"
    "v_sub_f32 v244, v10, v241\n\t"
    "v_sub_f32 v245, v11, v241\n\t"
    "v_exp_f32 v244, v244\n\t"
    "v_exp_f32 v245, v245\n\t"
    "s_nop 0\n\t"
    "v_add_f32 v224, v224, v244\n\t"
    "v_add_f32 v224, v224, v245\n\t"
    "v_cvt_pk_bf16_f32 v77, v244, v245\n\t"
    "v_sub_f32 v244, v12, v241\n\t"
    "v_sub_f32 v245, v13, v241\n\t"
    "v_exp_f32 v244, v244\n\t"
    "v_exp_f32 v245, v245\n\t"
    "s_nop 0\n\t"
    "v_add_f32 v224, v224, v244\n\t"
    "v_add_f32 v224, v224, v245\n\t"
    "v_cvt_pk_bf16_f32 v78, v244, v245\n\t"
    "v_sub_f32 v244, v14, v241\n\t"
    "v_sub_f32 v245, v15, v241\n\t"
    "v_exp_f32 v244, v244\n\t"
    "v_exp_f32 v245, v245\n\t"
    "s_nop 0\n\t"
    "v_add_f32 v224, v224, v244\n\t"
    "v_add_f32 v224, v224, v245\n\t"
    "v_cvt_pk_bf16_f32 v79, v244, v245\n\t"
    "v_max3_f32 v241, v64, v65, v66\n\t"
    "v_max_f32 v241, v241, v67\n\t"
    "v_mov_b32 v242, v241\n\t"
    "s_nop 1\n\t"
    "v_permlane16_swap_b32 v241, v242\n\t"
    "v_max_f32 v241, v241, v242\n\t"
    "v_mov_b32 v242, v241\n\t"
    "s_nop 1\n\t"
    "v_permlane32_swap_b32 v241, v242\n\t"
    "v_max_f32 v241, v241, v242\n\t"
    "v_ceil_f32 v241, v241\n\t"
    "v_max_f32 v241, 0, v241\n\t"
    "v_sub_f32 v243, 0, v241\n\t"
    "v_exp_f32 v243, v243\n\t"
    "v_sub_f32 v244, v68, v241\n\t"
    "v_mov_b32 v68, v244\n\t"
    "v_mov_b32 v69, v244\n\t"
    "v_mov_b32 v70, v244\n\t"
    "v_mov_b32 v71, v244\n\t"
    "v_mul_f32 v229, v229, v243\n\t"
    "v_accvgpr_read_b32 v244, a128\n\t"
    "v_mul_f32 v244, v244, v243\n\t"
    "v_accvgpr_write_b32 a128, v244\n\t"
    "v_accvgpr_read_b32 v245, a129\n\t"
    "v_mul_f32 v245, v245, v243\n\t"
    "v_accvgpr_write_b32 a129, v245\n\t"
    "v_accvgpr_read_b32 v244, a130\n\t"
    "v_mul_f32 v244, v244, v243\n\t"
    "v_accvgpr_write_b32 a130, v244\n\t"
    "v_accvgpr_read_b32 v245, a131\n\t"
    "v_mul_f32 v245, v245, v243\n\t"
    "v_accvgpr_write_b32 a131, v245\n\t"
    "v_accvgpr_read_b32 v244, a132\n\t"
    "v_mul_f32 v244, v244, v243\n\t"
    "v_accvgpr_write_b32 a132, v244\n\t"
    "v_accvgpr_read_b32 v245, a133\n\t"
    "v_mul_f32 v245, v245, v243\n\t"
    "v_accvgpr_write_b32 a133, v245\n\t"
    "v_accvgpr_read_b32 v244, a134\n\t"
    "v_mul_f32 v244, v244, v243\n\t"
    "v_accvgpr_write_b32 a134, v244\n\t"
    "v_accvgpr_read_b32 v245, a135\n\t"
    "v_mul_f32 v245, v245, v243\n\t"
    "v_accvgpr_write_b32 a135, v245\n\t"
    "v_accvgpr_read_b32 v244, a136\n\t"
    "v_mul_f32 v244, v244, v243\n\t"
    "v_accvgpr_write_b32 a136, v244\n\t"
    "v_accvgpr_read_b32 v245, a137\n\t"
    "v_mul_f32 v245, v245, v243\n\t"
    "v_accvgpr_write_b32 a137, v245\n\t"
    "v_accvgpr_read_b32 v244, a138\n\t"
    "v_mul_f32 v244, v244, v243\n\t"
    "v_accvgpr_write_b32 a138, v244\n\t"
    "v_accvgpr_read_b32 v245, a139\n\t"
    "v_mul_f32 v245, v245, v243\n\t"
    "v_accvgpr_write_b32 a139, v245\n\t"
    "v_accvgpr_read_b32 v244, a140\n\t"
    "v_mul_f32 v244, v244, v243\n\t"
    "v_accvgpr_write_b32 a140, v244\n\t"
    "v_accvgpr_read_b32 v245, a141\n\t"
    "v_mul_f32 v245, v245, v243\n\t"
    "v_accvgpr_write_b32 a141, v245\n\t"
    "v_accvgpr_read_b32 v244, a142\n\t"
    "v_mul_f32 v244, v244, v243\n\t"
    "v_accvgpr_write_b32 a142, v244\n\t"
    "v_accvgpr_read_b32 v245, a143\n\t"
    "v_mul_f32 v245, v245, v243\n\t"
    "v_accvgpr_write_b32 a143, v245\n\t"
    "v_accvgpr_read_b32 v244, a144\n\t"
    "v_mul_f32 v244, v244, v243\n\t"
    "v_accvgpr_write_b32 a144, v244\n\t"
    "v_accvgpr_read_b32 v245, a145\n\t"
    "v_mul_f32 v245, v245, v243\n\t"
    "v_accvgpr_write_b32 a145, v245\n\t"
    "v_accvgpr_read_b32 v244, a146\n\t"
    "v_mul_f32 v244, v244, v243\n\t"
    "v_accvgpr_write_b32 a146, v244\n\t"
    "v_accvgpr_read_b32 v245, a147\n\t"
    "v_mul_f32 v245, v245, v243\n\t"
    "v_accvgpr_write_b32 a147, v245\n\t"
    "v_accvgpr_read_b32 v244, a148\n\t"
    "v_mul_f32 v244, v244, v243\n\t"
    "v_accvgpr_write_b32 a148, v244\n\t"
    "v_accvgpr_read_b32 v245, a149\n\t"
    "v_mul_f32 v245, v245, v243\n\t"
    "v_accvgpr_write_b32 a149, v245\n\t"
    "v_accvgpr_read_b32 v244, a150\n\t"
    "v_mul_f32 v244, v244, v243\n\t"
    "v_accvgpr_write_b32 a150, v244\n\t"
    "v_accvgpr_read_b32 v245, a151\n\t"
    "v_mul_f32 v245, v245, v243\n\t"
    "v_accvgpr_write_b32 a151, v245\n\t"
    "v_accvgpr_read_b32 v244, a152\n\t"
    "v_mul_f32 v244, v244, v243\n\t"
    "v_accvgpr_write_b32 a152, v244\n\t"
    "v_accvgpr_read_b32 v245, a153\n\t"
    "v_mul_f32 v245, v245, v243\n\t"
    "v_accvgpr_write_b32 a153, v245\n\t"
    "v_accvgpr_read_b32 v244, a154\n\t"
    "v_mul_f32 v244, v244, v243\n\t"
    "v_accvgpr_write_b32 a154, v244\n\t"
    "v_accvgpr_read_b32 v245, a155\n\t"
    "v_mul_f32 v245, v245, v243\n\t"
    "v_accvgpr_write_b32 a155, v245\n\t"
    "v_accvgpr_read_b32 v244, a156\n\t"
    "v_mul_f32 v244, v244, v243\n\t"
    "v_accvgpr_write_b32 a156, v244\n\t"
    "v_accvgpr_read_b32 v245, a157\n\t"
    "v_mul_f32 v245, v245, v243\n\t"
    "v_accvgpr_write_b32 a157, v245\n\t"
    "v_accvgpr_read_b32 v244, a158\n\t"
    "v_mul_f32 v244, v244, v243\n\t"
    "v_accvgpr_write_b32 a158, v244\n\t"
    "v_accvgpr_read_b32 v245, a159\n\t"
    "v_mul_f32 v245, v245, v243\n\t"
    "v_accvgpr_write_b32 a159, v245\n\t"
    "v_lshlrev_b32 v244, 16, v96\n\t"
    "v_and_b32 v245, 0xffff0000, v96\n\t"
    "v_mul_f32 v244, v244, v243\n\t"
    "v_mul_f32 v245, v245, v243\n\t"
    "v_cvt_pk_bf16_f32 v96, v244, v245\n\t"
    "v_lshlrev_b32 v244, 16, v97\n\t"
    "v_and_b32 v245, 0xffff0000, v97\n\t"
    "v_mul_f32 v244, v244, v243\n\t"
    "v_mul_f32 v245, v245, v243\n\t"
    "v_cvt_pk_bf16_f32 v97, v244, v245\n\t"
    "v_sub_f32 v244, v64, v241\n\t"
    "v_sub_f32 v245, v65, v241\n\t"
    "v_exp_f32 v244, v244\n\t"
    "v_exp_f32 v245, v245\n\t"
    "s_nop 0\n\t"
    "v_add_f32 v226, v244, v245\n\t"
    "v_cvt_pk_bf16_f32 v98, v244, v245\n\t"
    "v_sub_f32 v244, v66, v241\n\t"
    "v_sub_f32 v245, v67, v241\n\t"
    "v_exp_f32 v244, v244\n\t"
    "v_exp_f32 v245, v245\n\t"
    "s_nop 0\n\t"
    "v_add_f32 v226, v226, v244\n\t"
    "v_add_f32 v226, v226, v245\n\t"
    "v_cvt_pk_bf16_f32 v99, v244, v245\n\t"
    "s_nop 1\n\t"
    "s_branch L_ret_c0e_%=\n\t"
    "L_rare_c1o_%=:\n\t"
    "s_nop 15\n\t"
    "v_max3_f32 v241, v16, v17, v18\n\t"
    "v_max3_f32 v241, v241, v19, v20\n\t"
    "v_max3_f32 v241, v241, v21, v22\n\t"
    "v_max3_f32 v241, v241, v23, v24\n\t"
    "v_max3_f32 v241, v241, v25, v26\n\t"
    "v_max3_f32 v241, v241, v27, v28\n\t"
    "v_max3_f32 v241, v241, v29, v30\n\t"
    "v_max_f32 v241, v241, v31\n\t"
    "v_mov_b32 v242, v241\n\t"
    "s_nop 1\n\t"
    "v_permlane32_swap_b32 v241, v242\n\t"
    "v_max_f32 v241, v241, v242\n\t"
    "v_ceil_f32 v241, v241\n\t"
    "v_max_f32 v241, 0, v241\n\t"
    "v_sub_f32 v243, 0, v241\n\t"
    "v_exp_f32 v243, v243\n\t"
    "v_sub_f32 v244, v48, v241\n\t"
    "v_mov_b32 v48, v244\n\t"
    "v_mov_b32 v49, v244\n\t"
    "v_mov_b32 v50, v244\n\t"
    "v_mov_b32 v51, v244\n\t"
    "v_mov_b32 v52, v244\n\t"
    "v_mov_b32 v53, v244\n\t"
    "v_mov_b32 v54, v244\n\t"
    "v_mov_b32 v55, v244\n\t"
    "v_mov_b32 v56, v244\n\t"
    "v_mov_b32 v57, v244\n\t"
    "v_mov_b32 v58, v244\n\t"
    "v_mov_b32 v59, v244\n\t"
    "v_mov_b32 v60, v244\n\t"
    "v_mov_b32 v61, v244\n\t"
    "v_mov_b32 v62, v244\n\t"
    "v_mov_b32 v63, v244\n\t"
    "v_mul_f32 v228, v228, v243\n\t"
    "v_accvgpr_read_b32 v244, a16\n\t"
    "v_mul_f32 v244, v244, v243\n\t"
    "v_accvgpr_write_b32 a16, v244\n\t"
    "v_accvgpr_read_b32 v245, a17\n\t"
    "v_mul_f32 v245, v245, v243\n\t"
    "v_accvgpr_write_b32 a17, v245\n\t"
    "v_accvgpr_read_b32 v244, a18\n\t"
    "v_mul_f32 v244, v244, v243\n\t"
    "v_accvgpr_write_b32 a18, v244\n\t"
    "v_accvgpr_read_b32 v245, a19\n\t"
    "v_mul_f32 v245, v245, v243\n\t"
    "v_accvgpr_write_b32 a19, v245\n\t"
    "v_accvgpr_read_b32 v244, a20\n\t"
    "v_mul_f32 v244, v244, v243\n\t"
    "v_accvgpr_write_b32 a20, v244\n\t"
    "v_accvgpr_read_b32 v245, a21\n\t"
    "v_mul_f32 v245, v245, v243\n\t"
    "v_accvgpr_write_b32 a21, v245\n\t"
    "v_accvgpr_read_b32 v244, a22\n\t"
    "v_mul_f32 v244, v244, v243\n\t"
    "v_accvgpr_write_b32 a22, v244\n\t"
    "v_accvgpr_read_b32 v245, a23\n\t"
    "v_mul_f32 v245, v245, v243\n\t"
    "v_accvgpr_write_b32 a23, v245\n\t"
    "v_accvgpr_read_b32 v244, a24\n\t"
    "v_mul_f32 v244, v244, v243\n\t"
    "v_accvgpr_write_b32 a24, v244\n\t"
    "v_accvgpr_read_b32 v245, a25\n\t"
    "v_mul_f32 v245, v245, v243\n\t"
    "v_accvgpr_write_b32 a25, v245\n\t"
    "v_accvgpr_read_b32 v244, a26\n\t"
    "v_mul_f32 v244, v244, v243\n\t"
    "v_accvgpr_write_b32 a26, v244\n\t"
    "v_accvgpr_read_b32 v245, a27\n\t"
    "v_mul_f32 v245, v245, v243\n\t"
    "v_accvgpr_write_b32 a27, v245\n\t"
    "v_accvgpr_read_b32 v244, a28\n\t"
    "v_mul_f32 v244, v244, v243\n\t"
    "v_accvgpr_write_b32 a28, v244\n\t"
    "v_accvgpr_read_b32 v245, a29\n\t"
    "v_mul_f32 v245, v245, v243\n\t"
    "v_accvgpr_write_b32 a29, v245\n\t"
    "v_accvgpr_read_b32 v244, a30\n\t"
    "v_mul_f32 v244, v244, v243\n\t"
    "v_accvgpr_write_b32 a30, v244\n\t"
    "v_accvgpr_read_b32 v245, a31\n\t"
    "v_mul_f32 v245, v245, v243\n\t"
    "v_accvgpr_write_b32 a31, v245\n\t"
    "v_accvgpr_read_b32 v244, a48\n\t"
    "v_mul_f32 v244, v244, v243\n\t"
    "v_accvgpr_write_b32 a48, v244\n\t"
    "v_accvgpr_read_b32 v245, a49\n\t"
    "v_mul_f32 v245, v245, v243\n\t"
    "v_accvgpr_write_b32 a49, v245\n\t"
    "v_accvgpr_read_b32 v244, a50\n\t"
    "v_mul_f32 v244, v244, v243\n\t"
    "v_accvgpr_write_b32 a50, v244\n\t"
    "v_accvgpr_read_b32 v245, a51\n\t"
    "v_mul_f32 v245, v245, v243\n\t"
    "v_accvgpr_write_b32 a51, v245\n\t"
    "v_accvgpr_read_b32 v244, a52\n\t"
    "v_mul_f32 v244, v244, v243\n\t"
    "v_accvgpr_write_b32 a52, v244\n\t"
    "v_accvgpr_read_b32 v245, a53\n\t"
    "v_mul_f32 v245, v245, v243\n\t"
    "v_accvgpr_write_b32 a53, v245\n\t"
    "v_accvgpr_read_b32 v244, a54\n\t"
    "v_mul_f32 v244, v244, v243\n\t"
    "v_accvgpr_write_b32 a54, v244\n\t"
    "v_accvgpr_read_b32 v245, a55\n\t"
    "v_mul_f32 v245, v245, v243\n\t"
    "v_accvgpr_write_b32 a55, v245\n\t"
    "v_accvgpr_read_b32 v244, a56\n\t"
    "v_mul_f32 v244, v244, v243\n\t"
    "v_accvgpr_write_b32 a56, v244\n\t"
    "v_accvgpr_read_b32 v245, a57\n\t"
    "v_mul_f32 v245, v245, v243\n\t"
    "v_accvgpr_write_b32 a57, v245\n\t"
    "v_accvgpr_read_b32 v244, a58\n\t"
    "v_mul_f32 v244, v244, v243\n\t"
    "v_accvgpr_write_b32 a58, v244\n\t"
    "v_accvgpr_read_b32 v245, a59\n\t"
    "v_mul_f32 v245, v245, v243\n\t"
    "v_accvgpr_write_b32 a59, v245\n\t"
    "v_accvgpr_read_b32 v244, a60\n\t"
    "v_mul_f32 v244, v244, v243\n\t"
    "v_accvgpr_write_b32 a60, v244\n\t"
    "v_accvgpr_read_b32 v245, a61\n\t"
    "v_mul_f32 v245, v245, v243\n\t"
    "v_accvgpr_write_b32 a61, v245\n\t"
    "v_accvgpr_read_b32 v244, a62\n\t"
    "v_mul_f32 v244, v244, v243\n\t"
    "v_accvgpr_write_b32 a62, v244\n\t"
    "v_accvgpr_read_b32 v245, a63\n\t"
    "v_mul_f32 v245, v245, v243\n\t"
    "v_accvgpr_write_b32 a63, v245\n\t"
    "v_accvgpr_read_b32 v244, a80\n\t"
    "v_mul_f32 v244, v244, v243\n\t"
    "v_accvgpr_write_b32 a80, v244\n\t"
    "v_accvgpr_read_b32 v245, a81\n\t"
    "v_mul_f32 v245, v245, v243\n\t"
    "v_accvgpr_write_b32 a81, v245\n\t"
    "v_accvgpr_read_b32 v244, a82\n\t"
    "v_mul_f32 v244, v244, v243\n\t"
    "v_accvgpr_write_b32 a82, v244\n\t"
    "v_accvgpr_read_b32 v245, a83\n\t"
    "v_mul_f32 v245, v245, v243\n\t"
    "v_accvgpr_write_b32 a83, v245\n\t"
    "v_accvgpr_read_b32 v244, a84\n\t"
    "v_mul_f32 v244, v244, v243\n\t"
    "v_accvgpr_write_b32 a84, v244\n\t"
    "v_accvgpr_read_b32 v245, a85\n\t"
    "v_mul_f32 v245, v245, v243\n\t"
    "v_accvgpr_write_b32 a85, v245\n\t"
    "v_accvgpr_read_b32 v244, a86\n\t"
    "v_mul_f32 v244, v244, v243\n\t"
    "v_accvgpr_write_b32 a86, v244\n\t"
    "v_accvgpr_read_b32 v245, a87\n\t"
    "v_mul_f32 v245, v245, v243\n\t"
    "v_accvgpr_write_b32 a87, v245\n\t"
    "v_accvgpr_read_b32 v244, a88\n\t"
    "v_mul_f32 v244, v244, v243\n\t"
    "v_accvgpr_write_b32 a88, v244\n\t"
    "v_accvgpr_read_b32 v245, a89\n\t"
    "v_mul_f32 v245, v245, v243\n\t"
    "v_accvgpr_write_b32 a89, v245\n\t"
    "v_accvgpr_read_b32 v244, a90\n\t"
    "v_mul_f32 v244, v244, v243\n\t"
    "v_accvgpr_write_b32 a90, v244\n\t"
    "v_accvgpr_read_b32 v245, a91\n\t"
    "v_mul_f32 v245, v245, v243\n\t"
    "v_accvgpr_write_b32 a91, v245\n\t"
    "v_accvgpr_read_b32 v244, a92\n\t"
    "v_mul_f32 v244, v244, v243\n\t"
    "v_accvgpr_write_b32 a92, v244\n\t"
    "v_accvgpr_read_b32 v245, a93\n\t"
    "v_mul_f32 v245, v245, v243\n\t"
    "v_accvgpr_write_b32 a93, v245\n\t"
    "v_accvgpr_read_b32 v244, a94\n\t"
    "v_mul_f32 v244, v244, v243\n\t"
    "v_accvgpr_write_b32 a94, v244\n\t"
    "v_accvgpr_read_b32 v245, a95\n\t"
    "v_mul_f32 v245, v245, v243\n\t"
    "v_accvgpr_write_b32 a95, v245\n\t"
    "v_accvgpr_read_b32 v244, a112\n\t"
    "v_mul_f32 v244, v244, v243\n\t"
    "v_accvgpr_write_b32 a112, v244\n\t"
    "v_accvgpr_read_b32 v245, a113\n\t"
    "v_mul_f32 v245, v245, v243\n\t"
    "v_accvgpr_write_b32 a113, v245\n\t"
    "v_accvgpr_read_b32 v244, a114\n\t"
    "v_mul_f32 v244, v244, v243\n\t"
    "v_accvgpr_write_b32 a114, v244\n\t"
    "v_accvgpr_read_b32 v245, a115\n\t"
    "v_mul_f32 v245, v245, v243\n\t"
    "v_accvgpr_write_b32 a115, v245\n\t"
    "v_accvgpr_read_b32 v244, a116\n\t"
    "v_mul_f32 v244, v244, v243\n\t"
    "v_accvgpr_write_b32 a116, v244\n\t"
    "v_accvgpr_read_b32 v245, a117\n\t"
    "v_mul_f32 v245, v245, v243\n\t"
    "v_accvgpr_write_b32 a117, v245\n\t"
    "v_accvgpr_read_b32 v244, a118\n\t"
    "v_mul_f32 v244, v244, v243\n\t"
    "v_accvgpr_write_b32 a118, v244\n\t"
    "v_accvgpr_read_b32 v245, a119\n\t"
    "v_mul_f32 v245, v245, v243\n\t"
    "v_accvgpr_write_b32 a119, v245\n\t"
    "v_accvgpr_read_b32 v244, a120\n\t"
    "v_mul_f32 v244, v244, v243\n\t"
    "v_accvgpr_write_b32 a120, v244\n\t"
    "v_accvgpr_read_b32 v245, a121\n\t"
    "v_mul_f32 v245, v245, v243\n\t"
    "v_accvgpr_write_b32 a121, v245\n\t"
    "v_accvgpr_read_b32 v244, a122\n\t"
    "v_mul_f32 v244, v244, v243\n\t"
    "v_accvgpr_write_b32 a122, v244\n\t"
    "v_accvgpr_read_b32 v245, a123\n\t"
    "v_mul_f32 v245, v245, v243\n\t"
    "v_accvgpr_write_b32 a123, v245\n\t"
    "v_accvgpr_read_b32 v244, a124\n\t"
    "v_mul_f32 v244, v244, v243\n\t"
    "v_accvgpr_write_b32 a124, v244\n\t"
    "v_accvgpr_read_b32 v245, a125\n\t"
    "v_mul_f32 v245, v245, v243\n\t"
    "v_accvgpr_write_b32 a125, v245\n\t"
    "v_accvgpr_read_b32 v244, a126\n\t"
    "v_mul_f32 v244, v244, v243\n\t"
    "v_accvgpr_write_b32 a126, v244\n\t"
    "v_accvgpr_read_b32 v245, a127\n\t"
    "v_mul_f32 v245, v245, v243\n\t"
    "v_accvgpr_write_b32 a127, v245\n\t"
    "v_sub_f32 v244, v16, v241\n\t"
    "v_sub_f32 v245, v17, v241\n\t"
    "v_exp_f32 v244, v244\n\t"
    "v_exp_f32 v245, v245\n\t"
    "s_nop 0\n\t"
    "v_add_f32 v225, v244, v245\n\t"
    "v_cvt_pk_bf16_f32 v88, v244, v245\n\t"
    "v_sub_f32 v244, v18, v241\n\t"
    "v_sub_f32 v245, v19, v241\n\t"
    "v_exp_f32 v244, v244\n\t"
    "v_exp_f32 v245, v245\n\t"
    "s_nop 0\n\t"
    "v_add_f32 v225, v225, v244\n\t"
    "v_add_f32 v225, v225, v245\n\t"
    "v_cvt_pk_bf16_f32 v89, v244, v245\n\t"
    "v_sub_f32 v244, v20, v241\n\t"
    "v_sub_f32 v245, v21, v241\n\t"
    "v_exp_f32 v244, v244\n\t"
    "v_exp_f32 v245, v245\n\t"
    "s_nop 0\n\t"
    "v_add_f32 v225, v225, v244\n\t"
    "v_add_f32 v225, v225, v245\n\t"
    "v_cvt_pk_bf16_f32 v90, v244, v245\n\t"
    "v_sub_f32 v244, v22, v241\n\t"
    "v_sub_f32 v245, v23, v241\n\t"
    "v_exp_f32 v244, v244\n\t"
    "v_exp_f32 v245, v245\n\t"
    "s_nop 0\n\t"
    "v_add_f32 v225, v225, v244\n\t"
    "v_add_f32 v225, v225, v245\n\t"
    "v_cvt_pk_bf16_f32 v91, v244, v245\n\t"
    "v_sub_f32 v244, v24, v241\n\t"
    "v_sub_f32 v245, v25, v241\n\t"
    "v_exp_f32 v244, v244\n\t"
    "v_exp_f32 v245, v245\n\t"
    "s_nop 0\n\t"
    "v_add_f32 v225, v225, v244\n\t"
    "v_add_f32 v225, v225, v245\n\t"
    "v_cvt_pk_bf16_f32 v92, v244, v245\n\t"
    "v_sub_f32 v244, v26, v241\n\t"
    "v_sub_f32 v245, v27, v241\n\t"
    "v_exp_f32 v244, v244\n\t"
    "v_exp_f32 v245, v245\n\t"
    "s_nop 0\n\t"
    "v_add_f32 v225, v225, v244\n\t"
    "v_add_f32 v225, v225, v245\n\t"
    "v_cvt_pk_bf16_f32 v93, v244, v245\n\t"
    "v_sub_f32 v244, v28, v241\n\t"
    "v_sub_f32 v245, v29, v241\n\t"
    "v_exp_f32 v244, v244\n\t"
    "v_exp_f32 v245, v245\n\t"
    "s_nop 0\n\t"
    "v_add_f32 v225, v225, v244\n\t"
    "v_add_f32 v225, v225, v245\n\t"
    "v_cvt_pk_bf16_f32 v94, v244, v245\n\t"
    "v_sub_f32 v244, v30, v241\n\t"
    "v_sub_f32 v245, v31, v241\n\t"
    "v_exp_f32 v244, v244\n\t"
    "v_exp_f32 v245, v245\n\t"
    "s_nop 0\n\t"
    "v_add_f32 v225, v225, v244\n\t"
    "v_add_f32 v225, v225, v245\n\t"
    "v_cvt_pk_bf16_f32 v95, v244, v245\n\t"
    "s_nop 1\n\t"
    "s_branch L_ret_c1o_%=\n\t"
    "L_rare_c0o_%=:\n\t"
    "s_nop 15\n\t"
    "v_max3_f32 v241, v0, v1, v2\n\t"
    "v_max3_f32 v241, v241, v3, v4\n\t"
    "v_max3_f32 v241, v241, v5, v6\n\t"
    "v_max3_f32 v241, v241, v7, v8\n\t"
    "v_max3_f32 v241, v241, v9, v10\n\t"
    "v_max3_f32 v241, v241, v11, v12\n\t"
    "v_max3_f32 v241, v241, v13, v14\n\t"
    "v_max_f32 v241, v241, v15\n\t"
    "v_mov_b32 v242, v241\n\t"
    "s_nop 1\n\t"
    "v_permlane32_swap_b32 v241, v242\n\t"
    "v_max_f32 v241, v241, v242\n\t"
    "v_ceil_f32 v241, v241\n\t"
    "v_max_f32 v241, 0, v241\n\t"
    "v_sub_f32 v243, 0, v241\n\t"
    "v_exp_f32 v243, v243\n\t"
    "v_sub_f32 v244, v32, v241\n\t"
    "v_mov_b32 v32, v244\n\t"
    "v_mov_b32 v33, v244\n\t"
    "v_mov_b32 v34, v244\n\t"
    "v_mov_b32 v35, v244\n\t"
    "v_mov_b32 v36, v244\n\t"
    "v_mov_b32 v37, v244\n\t"
    "v_mov_b32 v38, v244\n\t"
    "v_mov_b32 v39, v244\n\t"
    "v_mov_b32 v40, v244\n\t"
    "v_mov_b32 v41, v244\n\t"
    "v_mov_b32 v42, v244\n\t"
    "v_mov_b32 v43, v244\n\t"
    "v_mov_b32 v44, v244\n\t"
    "v_mov_b32 v45, v244\n\t"
    "v_mov_b32 v46, v244\n\t"
    "v_mov_b32 v47, v244\n\t"
    "v_mul_f32 v227, v227, v243\n\t"
    "v_accvgpr_read_b32 v244, a0\n\t"
    "v_mul_f32 v244, v244, v243\n\t"
    "v_accvgpr_write_b32 a0, v244\n\t"
    "v_accvgpr_read_b32 v245, a1\n\t"
    "v_mul_f32 v245, v245, v243\n\t"
    "v_accvgpr_write_b32 a1, v245\n\t"
    "v_accvgpr_read_b32 v244, a2\n\t"
    "v_mul_f32 v244, v244, v243\n\t"
    "v_accvgpr_write_b32 a2, v244\n\t"
    "v_accvgpr_read_b32 v245, a3\n\t"
    "v_mul_f32 v245, v245, v243\n\t"
    "v_accvgpr_write_b32 a3, v245\n\t"
    "v_accvgpr_read_b32 v244, a4\n\t"
    "v_mul_f32 v244, v244, v243\n\t"
    "v_accvgpr_write_b32 a4, v244\n\t"
    "v_accvgpr_read_b32 v245, a5\n\t"
    "v_mul_f32 v245, v245, v243\n\t"
    "v_accvgpr_write_b32 a5, v245\n\t"
    "v_accvgpr_read_b32 v244, a6\n\t"
    "v_mul_f32 v244, v244, v243\n\t"
    "v_accvgpr_write_b32 a6, v244\n\t"
    "v_accvgpr_read_b32 v245, a7\n\t"
    "v_mul_f32 v245, v245, v243\n\t"
    "v_accvgpr_write_b32 a7, v245\n\t"
    "v_accvgpr_read_b32 v244, a8\n\t"
    "v_mul_f32 v244, v244, v243\n\t"
    "v_accvgpr_write_b32 a8, v244\n\t"
    "v_accvgpr_read_b32 v245, a9\n\t"
    "v_mul_f32 v245, v245, v243\n\t"
    "v_accvgpr_write_b32 a9, v245\n\t"
    "v_accvgpr_read_b32 v244, a10\n\t"
    "v_mul_f32 v244, v244, v243\n\t"
    "v_accvgpr_write_b32 a10, v244\n\t"
    "v_accvgpr_read_b32 v245, a11\n\t"
    "v_mul_f32 v245, v245, v243\n\t"
    "v_accvgpr_write_b32 a11, v245\n\t"
    "v_accvgpr_read_b32 v244, a12\n\t"
    "v_mul_f32 v244, v244, v243\n\t"
    "v_accvgpr_write_b32 a12, v244\n\t"
    "v_accvgpr_read_b32 v245, a13\n\t"
    "v_mul_f32 v245, v245, v243\n\t"
    "v_accvgpr_write_b32 a13, v245\n\t"
    "v_accvgpr_read_b32 v244, a14\n\t"
    "v_mul_f32 v244, v244, v243\n\t"
    "v_accvgpr_write_b32 a14, v244\n\t"
    "v_accvgpr_read_b32 v245, a15\n\t"
    "v_mul_f32 v245, v245, v243\n\t"
    "v_accvgpr_write_b32 a15, v245\n\t"
    "v_accvgpr_read_b32 v244, a32\n\t"
    "v_mul_f32 v244, v244, v243\n\t"
    "v_accvgpr_write_b32 a32, v244\n\t"
    "v_accvgpr_read_b32 v245, a33\n\t"
    "v_mul_f32 v245, v245, v243\n\t"
    "v_accvgpr_write_b32 a33, v245\n\t"
    "v_accvgpr_read_b32 v244, a34\n\t"
    "v_mul_f32 v244, v244, v243\n\t"
    "v_accvgpr_write_b32 a34, v244\n\t"
    "v_accvgpr_read_b32 v245, a35\n\t"
    "v_mul_f32 v245, v245, v243\n\t"
    "v_accvgpr_write_b32 a35, v245\n\t"
    "v_accvgpr_read_b32 v244, a36\n\t"
    "v_mul_f32 v244, v244, v243\n\t"
    "v_accvgpr_write_b32 a36, v244\n\t"
    "v_accvgpr_read_b32 v245, a37\n\t"
    "v_mul_f32 v245, v245, v243\n\t"
    "v_accvgpr_write_b32 a37, v245\n\t"
    "v_accvgpr_read_b32 v244, a38\n\t"
    "v_mul_f32 v244, v244, v243\n\t"
    "v_accvgpr_write_b32 a38, v244\n\t"
    "v_accvgpr_read_b32 v245, a39\n\t"
    "v_mul_f32 v245, v245, v243\n\t"
    "v_accvgpr_write_b32 a39, v245\n\t"
    "v_accvgpr_read_b32 v244, a40\n\t"
    "v_mul_f32 v244, v244, v243\n\t"
    "v_accvgpr_write_b32 a40, v244\n\t"
    "v_accvgpr_read_b32 v245, a41\n\t"
    "v_mul_f32 v245, v245, v243\n\t"
    "v_accvgpr_write_b32 a41, v245\n\t"
    "v_accvgpr_read_b32 v244, a42\n\t"
    "v_mul_f32 v244, v244, v243\n\t"
    "v_accvgpr_write_b32 a42, v244\n\t"
    "v_accvgpr_read_b32 v245, a43\n\t"
    "v_mul_f32 v245, v245, v243\n\t"
    "v_accvgpr_write_b32 a43, v245\n\t"
    "v_accvgpr_read_b32 v244, a44\n\t"
    "v_mul_f32 v244, v244, v243\n\t"
    "v_accvgpr_write_b32 a44, v244\n\t"
    "v_accvgpr_read_b32 v245, a45\n\t"
    "v_mul_f32 v245, v245, v243\n\t"
    "v_accvgpr_write_b32 a45, v245\n\t"
    "v_accvgpr_read_b32 v244, a46\n\t"
    "v_mul_f32 v244, v244, v243\n\t"
    "v_accvgpr_write_b32 a46, v244\n\t"
    "v_accvgpr_read_b32 v245, a47\n\t"
    "v_mul_f32 v245, v245, v243\n\t"
    "v_accvgpr_write_b32 a47, v245\n\t"
    "v_accvgpr_read_b32 v244, a64\n\t"
    "v_mul_f32 v244, v244, v243\n\t"
    "v_accvgpr_write_b32 a64, v244\n\t"
    "v_accvgpr_read_b32 v245, a65\n\t"
    "v_mul_f32 v245, v245, v243\n\t"
    "v_accvgpr_write_b32 a65, v245\n\t"
    "v_accvgpr_read_b32 v244, a66\n\t"
    "v_mul_f32 v244, v244, v243\n\t"
    "v_accvgpr_write_b32 a66, v244\n\t"
    "v_accvgpr_read_b32 v245, a67\n\t"
    "v_mul_f32 v245, v245, v243\n\t"
    "v_accvgpr_write_b32 a67, v245\n\t"
    "v_accvgpr_read_b32 v244, a68\n\t"
    "v_mul_f32 v244, v244, v243\n\t"
    "v_accvgpr_write_b32 a68, v244\n\t"
    "v_accvgpr_read_b32 v245, a69\n\t"
    "v_mul_f32 v245, v245, v243\n\t"
    "v_accvgpr_write_b32 a69, v245\n\t"
    "v_accvgpr_read_b32 v244, a70\n\t"
    "v_mul_f32 v244, v244, v243\n\t"
    "v_accvgpr_write_b32 a70, v244\n\t"
    "v_accvgpr_read_b32 v245, a71\n\t"
    "v_mul_f32 v245, v245, v243\n\t"
    "v_accvgpr_write_b32 a71, v245\n\t"
    "v_accvgpr_read_b32 v244, a72\n\t"
    "v_mul_f32 v244, v244, v243\n\t"
    "v_accvgpr_write_b32 a72, v244\n\t"
    "v_accvgpr_read_b32 v245, a73\n\t"
    "v_mul_f32 v245, v245, v243\n\t"
    "v_accvgpr_write_b32 a73, v245\n\t"
    "v_accvgpr_read_b32 v244, a74\n\t"
    "v_mul_f32 v244, v244, v243\n\t"
    "v_accvgpr_write_b32 a74, v244\n\t"
    "v_accvgpr_read_b32 v245, a75\n\t"
    "v_mul_f32 v245, v245, v243\n\t"
    "v_accvgpr_write_b32 a75, v245\n\t"
    "v_accvgpr_read_b32 v244, a76\n\t"
    "v_mul_f32 v244, v244, v243\n\t"
    "v_accvgpr_write_b32 a76, v244\n\t"
    "v_accvgpr_read_b32 v245, a77\n\t"
    "v_mul_f32 v245, v245, v243\n\t"
    "v_accvgpr_write_b32 a77, v245\n\t"
    "v_accvgpr_read_b32 v244, a78\n\t"
    "v_mul_f32 v244, v244, v243\n\t"
    "v_accvgpr_write_b32 a78, v244\n\t"
    "v_accvgpr_read_b32 v245, a79\n\t"
    "v_mul_f32 v245, v245, v243\n\t"
    "v_accvgpr_write_b32 a79, v245\n\t"
    "v_accvgpr_read_b32 v244, a96\n\t"
    "v_mul_f32 v244, v244, v243\n\t"
    "v_accvgpr_write_b32 a96, v244\n\t"
    "v_accvgpr_read_b32 v245, a97\n\t"
    "v_mul_f32 v245, v245, v243\n\t"
    "v_accvgpr_write_b32 a97, v245\n\t"
    "v_accvgpr_read_b32 v244, a98\n\t"
    "v_mul_f32 v244, v244, v243\n\t"
    "v_accvgpr_write_b32 a98, v244\n\t"
    "v_accvgpr_read_b32 v245, a99\n\t"
    "v_mul_f32 v245, v245, v243\n\t"
    "v_accvgpr_write_b32 a99, v245\n\t"
    "v_accvgpr_read_b32 v244, a100\n\t"
    "v_mul_f32 v244, v244, v243\n\t"
    "v_accvgpr_write_b32 a100, v244\n\t"
    "v_accvgpr_read_b32 v245, a101\n\t"
    "v_mul_f32 v245, v245, v243\n\t"
    "v_accvgpr_write_b32 a101, v245\n\t"
    "v_accvgpr_read_b32 v244, a102\n\t"
    "v_mul_f32 v244, v244, v243\n\t"
    "v_accvgpr_write_b32 a102, v244\n\t"
    "v_accvgpr_read_b32 v245, a103\n\t"
    "v_mul_f32 v245, v245, v243\n\t"
    "v_accvgpr_write_b32 a103, v245\n\t"
    "v_accvgpr_read_b32 v244, a104\n\t"
    "v_mul_f32 v244, v244, v243\n\t"
    "v_accvgpr_write_b32 a104, v244\n\t"
    "v_accvgpr_read_b32 v245, a105\n\t"
    "v_mul_f32 v245, v245, v243\n\t"
    "v_accvgpr_write_b32 a105, v245\n\t"
    "v_accvgpr_read_b32 v244, a106\n\t"
    "v_mul_f32 v244, v244, v243\n\t"
    "v_accvgpr_write_b32 a106, v244\n\t"
    "v_accvgpr_read_b32 v245, a107\n\t"
    "v_mul_f32 v245, v245, v243\n\t"
    "v_accvgpr_write_b32 a107, v245\n\t"
    "v_accvgpr_read_b32 v244, a108\n\t"
    "v_mul_f32 v244, v244, v243\n\t"
    "v_accvgpr_write_b32 a108, v244\n\t"
    "v_accvgpr_read_b32 v245, a109\n\t"
    "v_mul_f32 v245, v245, v243\n\t"
    "v_accvgpr_write_b32 a109, v245\n\t"
    "v_accvgpr_read_b32 v244, a110\n\t"
    "v_mul_f32 v244, v244, v243\n\t"
    "v_accvgpr_write_b32 a110, v244\n\t"
    "v_accvgpr_read_b32 v245, a111\n\t"
    "v_mul_f32 v245, v245, v243\n\t"
    "v_accvgpr_write_b32 a111, v245\n\t"
    "v_sub_f32 v244, v0, v241\n\t"
    "v_sub_f32 v245, v1, v241\n\t"
    "v_exp_f32 v244, v244\n\t"
    "v_exp_f32 v245, v245\n\t"
    "s_nop 0\n\t"
    "v_add_f32 v224, v244, v245\n\t"
    "v_cvt_pk_bf16_f32 v80, v244, v245\n\t"
    "v_sub_f32 v244, v2, v241\n\t"
    "v_sub_f32 v245, v3, v241\n\t"
    "v_exp_f32 v244, v244\n\t"
    "v_exp_f32 v245, v245\n\t"
    "s_nop 0\n\t"
    "v_add_f32 v224, v224, v244\n\t"
    "v_add_f32 v224, v224, v245\n\t"
    "v_cvt_pk_bf16_f32 v81, v244, v245\n\t"
    "v_sub_f32 v244, v4, v241\n\t"
    "v_sub_f32 v245, v5, v241\n\t"
    "v_exp_f32 v244, v244\n\t"
    "v_exp_f32 v245, v245\n\t"
    "s_nop 0\n\t"
    "v_add_f32 v224, v224, v244\n\t"
    "v_add_f32 v224, v224, v245\n\t"
    "v_cvt_pk_bf16_f32 v82, v244, v245\n\t"
    "v_sub_f32 v244, v6, v241\n\t"
    "v_sub_f32 v245, v7, v241\n\t"
    "v_exp_f32 v244, v244\n\t"
    "v_exp_f32 v245, v245\n\t"
    "s_nop 0\n\t"
    "v_add_f32 v224, v224, v244\n\t"
    "v_add_f32 v224, v224, v245\n\t"
    "v_cvt_pk_bf16_f32 v83, v244, v245\n\t"
    "v_sub_f32 v244, v8, v241\n\t"
    "v_sub_f32 v245, v9, v241\n\t"
    "v_exp_f32 v244, v244\n\t"
    "v_exp_f32 v245, v245\n\t"
    "s_nop 0\n\t"
    "v_add_f32 v224, v224, v244\n\t"
    "v_add_f32 v224, v224, v245\n\t"
    "v_cvt_pk_bf16_f32 v84, v244, v245\n\t"
    "v_sub_f32 v244, v10, v241\n\t"
    "v_sub_f32 v245, v11, v241\n\t"
    "v_exp_f32 v244, v244\n\t"
    "v_exp_f32 v245, v245\n\t"
    "s_nop 0\n\t"
    "v_add_f32 v224, v224, v244\n\t"
    "v_add_f32 v224, v224, v245\n\t"
    "v_cvt_pk_bf16_f32 v85, v244, v245\n\t"
    "v_sub_f32 v244, v12, v241\n\t"
    "v_sub_f32 v245, v13, v241\n\t"
    "v_exp_f32 v244, v244\n\t"
    "v_exp_f32 v245, v245\n\t"
    "s_nop 0\n\t"
    "v_add_f32 v224, v224, v244\n\t"
    "v_add_f32 v224, v224, v245\n\t"
    "v_cvt_pk_bf16_f32 v86, v244, v245\n\t"
    "v_sub_f32 v244, v14, v241\n\t"
    "v_sub_f32 v245, v15, v241\n\t"
    "v_exp_f32 v244, v244\n\t"
    "v_exp_f32 v245, v245\n\t"
    "s_nop 0\n\t"
    "v_add_f32 v224, v224, v244\n\t"
    "v_add_f32 v224, v224, v245\n\t"
    "v_cvt_pk_bf16_f32 v87, v244, v245\n\t"
    "v_max3_f32 v241, v64, v65, v66\n\t"
    "v_max_f32 v241, v241, v67\n\t"
    "v_mov_b32 v242, v241\n\t"
    "s_nop 1\n\t"
    "v_permlane16_swap_b32 v241, v242\n\t"
    "v_max_f32 v241, v241, v242\n\t"
    "v_mov_b32 v242, v241\n\t"
    "s_nop 1\n\t"
    "v_permlane32_swap_b32 v241, v242\n\t"
    "v_max_f32 v241, v241, v242\n\t"
    "v_ceil_f32 v241, v241\n\t"
    "v_max_f32 v241, 0, v241\n\t"
    "v_sub_f32 v243, 0, v241\n\t"
    "v_exp_f32 v243, v243\n\t"
    "v_sub_f32 v244, v68, v241\n\t"
    "v_mov_b32 v68, v244\n\t"
    "v_mov_b32 v69, v244\n\t"
    "v_mov_b32 v70, v244\n\t"
    "v_mov_b32 v71, v244\n\t"
    "v_mul_f32 v229, v229, v243\n\t"
    "v_accvgpr_read_b32 v244, a128\n\t"
    "v_mul_f32 v244, v244, v243\n\t"
    "v_accvgpr_write_b32 a128, v244\n\t"
    "v_accvgpr_read_b32 v245, a129\n\t"
    "v_mul_f32 v245, v245, v243\n\t"
    "v_accvgpr_write_b32 a129, v245\n\t"
    "v_accvgpr_read_b32 v244, a130\n\t"
    "v_mul_f32 v244, v244, v243\n\t"
    "v_accvgpr_write_b32 a130, v244\n\t"
    "v_accvgpr_read_b32 v245, a131\n\t"
    "v_mul_f32 v245, v245, v243\n\t"
    "v_accvgpr_write_b32 a131, v245\n\t"
    "v_accvgpr_read_b32 v244, a132\n\t"
    "v_mul_f32 v244, v244, v243\n\t"
    "v_accvgpr_write_b32 a132, v244\n\t"
    "v_accvgpr_read_b32 v245, a133\n\t"
    "v_mul_f32 v245, v245, v243\n\t"
    "v_accvgpr_write_b32 a133, v245\n\t"
    "v_accvgpr_read_b32 v244, a134\n\t"
    "v_mul_f32 v244, v244, v243\n\t"
    "v_accvgpr_write_b32 a134, v244\n\t"
    "v_accvgpr_read_b32 v245, a135\n\t"
    "v_mul_f32 v245, v245, v243\n\t"
    "v_accvgpr_write_b32 a135, v245\n\t"
    "v_accvgpr_read_b32 v244, a136\n\t"
    "v_mul_f32 v244, v244, v243\n\t"
    "v_accvgpr_write_b32 a136, v244\n\t"
    "v_accvgpr_read_b32 v245, a137\n\t"
    "v_mul_f32 v245, v245, v243\n\t"
    "v_accvgpr_write_b32 a137, v245\n\t"
    "v_accvgpr_read_b32 v244, a138\n\t"
    "v_mul_f32 v244, v244, v243\n\t"
    "v_accvgpr_write_b32 a138, v244\n\t"
    "v_accvgpr_read_b32 v245, a139\n\t"
    "v_mul_f32 v245, v245, v243\n\t"
    "v_accvgpr_write_b32 a139, v245\n\t"
    "v_accvgpr_read_b32 v244, a140\n\t"
    "v_mul_f32 v244, v244, v243\n\t"
    "v_accvgpr_write_b32 a140, v244\n\t"
    "v_accvgpr_read_b32 v245, a141\n\t"
    "v_mul_f32 v245, v245, v243\n\t"
    "v_accvgpr_write_b32 a141, v245\n\t"
    "v_accvgpr_read_b32 v244, a142\n\t"
    "v_mul_f32 v244, v244, v243\n\t"
    "v_accvgpr_write_b32 a142, v244\n\t"
    "v_accvgpr_read_b32 v245, a143\n\t"
    "v_mul_f32 v245, v245, v243\n\t"
    "v_accvgpr_write_b32 a143, v245\n\t"
    "v_accvgpr_read_b32 v244, a144\n\t"
    "v_mul_f32 v244, v244, v243\n\t"
    "v_accvgpr_write_b32 a144, v244\n\t"
    "v_accvgpr_read_b32 v245, a145\n\t"
    "v_mul_f32 v245, v245, v243\n\t"
    "v_accvgpr_write_b32 a145, v245\n\t"
    "v_accvgpr_read_b32 v244, a146\n\t"
    "v_mul_f32 v244, v244, v243\n\t"
    "v_accvgpr_write_b32 a146, v244\n\t"
    "v_accvgpr_read_b32 v245, a147\n\t"
    "v_mul_f32 v245, v245, v243\n\t"
    "v_accvgpr_write_b32 a147, v245\n\t"
    "v_accvgpr_read_b32 v244, a148\n\t"
    "v_mul_f32 v244, v244, v243\n\t"
    "v_accvgpr_write_b32 a148, v244\n\t"
    "v_accvgpr_read_b32 v245, a149\n\t"
    "v_mul_f32 v245, v245, v243\n\t"
    "v_accvgpr_write_b32 a149, v245\n\t"
    "v_accvgpr_read_b32 v244, a150\n\t"
    "v_mul_f32 v244, v244, v243\n\t"
    "v_accvgpr_write_b32 a150, v244\n\t"
    "v_accvgpr_read_b32 v245, a151\n\t"
    "v_mul_f32 v245, v245, v243\n\t"
    "v_accvgpr_write_b32 a151, v245\n\t"
    "v_accvgpr_read_b32 v244, a152\n\t"
    "v_mul_f32 v244, v244, v243\n\t"
    "v_accvgpr_write_b32 a152, v244\n\t"
    "v_accvgpr_read_b32 v245, a153\n\t"
    "v_mul_f32 v245, v245, v243\n\t"
    "v_accvgpr_write_b32 a153, v245\n\t"
    "v_accvgpr_read_b32 v244, a154\n\t"
    "v_mul_f32 v244, v244, v243\n\t"
    "v_accvgpr_write_b32 a154, v244\n\t"
    "v_accvgpr_read_b32 v245, a155\n\t"
    "v_mul_f32 v245, v245, v243\n\t"
    "v_accvgpr_write_b32 a155, v245\n\t"
    "v_accvgpr_read_b32 v244, a156\n\t"
    "v_mul_f32 v244, v244, v243\n\t"
    "v_accvgpr_write_b32 a156, v244\n\t"
    "v_accvgpr_read_b32 v245, a157\n\t"
    "v_mul_f32 v245, v245, v243\n\t"
    "v_accvgpr_write_b32 a157, v245\n\t"
    "v_accvgpr_read_b32 v244, a158\n\t"
    "v_mul_f32 v244, v244, v243\n\t"
    "v_accvgpr_write_b32 a158, v244\n\t"
    "v_accvgpr_read_b32 v245, a159\n\t"
    "v_mul_f32 v245, v245, v243\n\t"
    "v_accvgpr_write_b32 a159, v245\n\t"
    "v_sub_f32 v244, v64, v241\n\t"
    "v_sub_f32 v245, v65, v241\n\t"
    "v_exp_f32 v244, v244\n\t"
    "v_exp_f32 v245, v245\n\t"
    "s_nop 0\n\t"
    "v_add_f32 v226, v244, v245\n\t"
    "v_cvt_pk_bf16_f32 v96, v244, v245\n\t"
    "v_sub_f32 v244, v66, v241\n\t"
    "v_sub_f32 v245, v67, v241\n\t"
    "v_exp_f32 v244, v244\n\t"
    "v_exp_f32 v245, v245\n\t"
    "s_nop 0\n\t"
    "v_add_f32 v226, v226, v244\n\t"
    "v_add_f32 v226, v226, v245\n\t"
    "v_cvt_pk_bf16_f32 v97, v244, v245\n\t"
    "s_nop 1\n\t"
    "s_branch L_ret_c0o_%=\n\t"
    "L_rare_c1t_%=:\n\t"
    "s_nop 15\n\t"
    "v_max3_f32 v241, v16, v17, v18\n\t"
    "v_max3_f32 v241, v241, v19, v20\n\t"
    "v_max3_f32 v241, v241, v21, v22\n\t"
    "v_max3_f32 v241, v241, v23, v24\n\t"
    "v_max3_f32 v241, v241, v25, v26\n\t"
    "v_max3_f32 v241, v241, v27, v28\n\t"
    "v_max3_f32 v241, v241, v29, v30\n\t"
    "v_max_f32 v241, v241, v31\n\t"
    "v_mov_b32 v242, v241\n\t"
    "s_nop 1\n\t"
    "v_permlane32_swap_b32 v241, v242\n\t"
    "v_max_f32 v241, v241, v242\n\t"
    "v_ceil_f32 v241, v241\n\t"
    "v_max_f32 v241, 0, v241\n\t"
    "v_sub_f32 v243, 0, v241\n\t"
    "v_exp_f32 v243, v243\n\t"
    "v_sub_f32 v244, v48, v241\n\t"
    "v_mov_b32 v48, v244\n\t"
    "v_mov_b32 v49, v244\n\t"
    "v_mov_b32 v50, v244\n\t"
    "v_mov_b32 v51, v244\n\t"
    "v_mov_b32 v52, v244\n\t"
    "v_mov_b32 v53, v244\n\t"
    "v_mov_b32 v54, v244\n\t"
    "v_mov_b32 v55, v244\n\t"
    "v_mov_b32 v56, v244\n\t"
    "v_mov_b32 v57, v244\n\t"
    "v_mov_b32 v58, v244\n\t"
    "v_mov_b32 v59, v244\n\t"
    "v_mov_b32 v60, v244\n\t"
    "v_mov_b32 v61, v244\n\t"
    "v_mov_b32 v62, v244\n\t"
    "v_mov_b32 v63, v244\n\t"
    "v_mul_f32 v228, v228, v243\n\t"
    "v_accvgpr_read_b32 v244, a16\n\t"
    "v_mul_f32 v244, v244, v243\n\t"
    "v_accvgpr_write_b32 a16, v244\n\t"
    "v_accvgpr_read_b32 v245, a17\n\t"
    "v_mul_f32 v245, v245, v243\n\t"
    "v_accvgpr_write_b32 a17, v245\n\t"
    "v_accvgpr_read_b32 v244, a18\n\t"
    "v_mul_f32 v244, v244, v243\n\t"
    "v_accvgpr_write_b32 a18, v244\n\t"
    "v_accvgpr_read_b32 v245, a19\n\t"
    "v_mul_f32 v245, v245, v243\n\t"
    "v_accvgpr_write_b32 a19, v245\n\t"
    "v_accvgpr_read_b32 v244, a20\n\t"
    "v_mul_f32 v244, v244, v243\n\t"
    "v_accvgpr_write_b32 a20, v244\n\t"
    "v_accvgpr_read_b32 v245, a21\n\t"
    "v_mul_f32 v245, v245, v243\n\t"
    "v_accvgpr_write_b32 a21, v245\n\t"
    "v_accvgpr_read_b32 v244, a22\n\t"
    "v_mul_f32 v244, v244, v243\n\t"
    "v_accvgpr_write_b32 a22, v244\n\t"
    "v_accvgpr_read_b32 v245, a23\n\t"
    "v_mul_f32 v245, v245, v243\n\t"
    "v_accvgpr_write_b32 a23, v245\n\t"
    "v_accvgpr_read_b32 v244, a24\n\t"
    "v_mul_f32 v244, v244, v243\n\t"
    "v_accvgpr_write_b32 a24, v244\n\t"
    "v_accvgpr_read_b32 v245, a25\n\t"
    "v_mul_f32 v245, v245, v243\n\t"
    "v_accvgpr_write_b32 a25, v245\n\t"
    "v_accvgpr_read_b32 v244, a26\n\t"
    "v_mul_f32 v244, v244, v243\n\t"
    "v_accvgpr_write_b32 a26, v244\n\t"
    "v_accvgpr_read_b32 v245, a27\n\t"
    "v_mul_f32 v245, v245, v243\n\t"
    "v_accvgpr_write_b32 a27, v245\n\t"
    "v_accvgpr_read_b32 v244, a28\n\t"
    "v_mul_f32 v244, v244, v243\n\t"
    "v_accvgpr_write_b32 a28, v244\n\t"
    "v_accvgpr_read_b32 v245, a29\n\t"
    "v_mul_f32 v245, v245, v243\n\t"
    "v_accvgpr_write_b32 a29, v245\n\t"
    "v_accvgpr_read_b32 v244, a30\n\t"
    "v_mul_f32 v244, v244, v243\n\t"
    "v_accvgpr_write_b32 a30, v244\n\t"
    "v_accvgpr_read_b32 v245, a31\n\t"
    "v_mul_f32 v245, v245, v243\n\t"
    "v_accvgpr_write_b32 a31, v245\n\t"
    "v_accvgpr_read_b32 v244, a48\n\t"
    "v_mul_f32 v244, v244, v243\n\t"
    "v_accvgpr_write_b32 a48, v244\n\t"
    "v_accvgpr_read_b32 v245, a49\n\t"
    "v_mul_f32 v245, v245, v243\n\t"
    "v_accvgpr_write_b32 a49, v245\n\t"
    "v_accvgpr_read_b32 v244, a50\n\t"
    "v_mul_f32 v244, v244, v243\n\t"
    "v_accvgpr_write_b32 a50, v244\n\t"
    "v_accvgpr_read_b32 v245, a51\n\t"
    "v_mul_f32 v245, v245, v243\n\t"
    "v_accvgpr_write_b32 a51, v245\n\t"
    "v_accvgpr_read_b32 v244, a52\n\t"
    "v_mul_f32 v244, v244, v243\n\t"
    "v_accvgpr_write_b32 a52, v244\n\t"
    "v_accvgpr_read_b32 v245, a53\n\t"
    "v_mul_f32 v245, v245, v243\n\t"
    "v_accvgpr_write_b32 a53, v245\n\t"
    "v_accvgpr_read_b32 v244, a54\n\t"
    "v_mul_f32 v244, v244, v243\n\t"
    "v_accvgpr_write_b32 a54, v244\n\t"
    "v_accvgpr_read_b32 v245, a55\n\t"
    "v_mul_f32 v245, v245, v243\n\t"
    "v_accvgpr_write_b32 a55, v245\n\t"
    "v_accvgpr_read_b32 v244, a56\n\t"
    "v_mul_f32 v244, v244, v243\n\t"
    "v_accvgpr_write_b32 a56, v244\n\t"
    "v_accvgpr_read_b32 v245, a57\n\t"
    "v_mul_f32 v245, v245, v243\n\t"
    "v_accvgpr_write_b32 a57, v245\n\t"
    "v_accvgpr_read_b32 v244, a58\n\t"
    "v_mul_f32 v244, v244, v243\n\t"
    "v_accvgpr_write_b32 a58, v244\n\t"
    "v_accvgpr_read_b32 v245, a59\n\t"
    "v_mul_f32 v245, v245, v243\n\t"
    "v_accvgpr_write_b32 a59, v245\n\t"
    "v_accvgpr_read_b32 v244, a60\n\t"
    "v_mul_f32 v244, v244, v243\n\t"
    "v_accvgpr_write_b32 a60, v244\n\t"
    "v_accvgpr_read_b32 v245, a61\n\t"
    "v_mul_f32 v245, v245, v243\n\t"
    "v_accvgpr_write_b32 a61, v245\n\t"
    "v_accvgpr_read_b32 v244, a62\n\t"
    "v_mul_f32 v244, v244, v243\n\t"
    "v_accvgpr_write_b32 a62, v244\n\t"
    "v_accvgpr_read_b32 v245, a63\n\t"
    "v_mul_f32 v245, v245, v243\n\t"
    "v_accvgpr_write_b32 a63, v245\n\t"
    "v_accvgpr_read_b32 v244, a80\n\t"
    "v_mul_f32 v244, v244, v243\n\t"
    "v_accvgpr_write_b32 a80, v244\n\t"
    "v_accvgpr_read_b32 v245, a81\n\t"
    "v_mul_f32 v245, v245, v243\n\t"
    "v_accvgpr_write_b32 a81, v245\n\t"
    "v_accvgpr_read_b32 v244, a82\n\t"
    "v_mul_f32 v244, v244, v243\n\t"
    "v_accvgpr_write_b32 a82, v244\n\t"
    "v_accvgpr_read_b32 v245, a83\n\t"
    "v_mul_f32 v245, v245, v243\n\t"
    "v_accvgpr_write_b32 a83, v245\n\t"
    "v_accvgpr_read_b32 v244, a84\n\t"
    "v_mul_f32 v244, v244, v243\n\t"
    "v_accvgpr_write_b32 a84, v244\n\t"
    "v_accvgpr_read_b32 v245, a85\n\t"
    "v_mul_f32 v245, v245, v243\n\t"
    "v_accvgpr_write_b32 a85, v245\n\t"
    "v_accvgpr_read_b32 v244, a86\n\t"
    "v_mul_f32 v244, v244, v243\n\t"
    "v_accvgpr_write_b32 a86, v244\n\t"
    "v_accvgpr_read_b32 v245, a87\n\t"
    "v_mul_f32 v245, v245, v243\n\t"
    "v_accvgpr_write_b32 a87, v245\n\t"
    "v_accvgpr_read_b32 v244, a88\n\t"
    "v_mul_f32 v244, v244, v243\n\t"
    "v_accvgpr_write_b32 a88, v244\n\t"
    "v_accvgpr_read_b32 v245, a89\n\t"
    "v_mul_f32 v245, v245, v243\n\t"
    "v_accvgpr_write_b32 a89, v245\n\t"
    "v_accvgpr_read_b32 v244, a90\n\t"
    "v_mul_f32 v244, v244, v243\n\t"
    "v_accvgpr_write_b32 a90, v244\n\t"
    "v_accvgpr_read_b32 v245, a91\n\t"
    "v_mul_f32 v245, v245, v243\n\t"
    "v_accvgpr_write_b32 a91, v245\n\t"
    "v_accvgpr_read_b32 v244, a92\n\t"
    "v_mul_f32 v244, v244, v243\n\t"
    "v_accvgpr_write_b32 a92, v244\n\t"
    "v_accvgpr_read_b32 v245, a93\n\t"
    "v_mul_f32 v245, v245, v243\n\t"
    "v_accvgpr_write_b32 a93, v245\n\t"
    "v_accvgpr_read_b32 v244, a94\n\t"
    "v_mul_f32 v244, v244, v243\n\t"
    "v_accvgpr_write_b32 a94, v244\n\t"
    "v_accvgpr_read_b32 v245, a95\n\t"
    "v_mul_f32 v245, v245, v243\n\t"
    "v_accvgpr_write_b32 a95, v245\n\t"
    "v_accvgpr_read_b32 v244, a112\n\t"
    "v_mul_f32 v244, v244, v243\n\t"
    "v_accvgpr_write_b32 a112, v244\n\t"
    "v_accvgpr_read_b32 v245, a113\n\t"
    "v_mul_f32 v245, v245, v243\n\t"
    "v_accvgpr_write_b32 a113, v245\n\t"
    "v_accvgpr_read_b32 v244, a114\n\t"
    "v_mul_f32 v244, v244, v243\n\t"
    "v_accvgpr_write_b32 a114, v244\n\t"
    "v_accvgpr_read_b32 v245, a115\n\t"
    "v_mul_f32 v245, v245, v243\n\t"
    "v_accvgpr_write_b32 a115, v245\n\t"
    "v_accvgpr_read_b32 v244, a116\n\t"
    "v_mul_f32 v244, v244, v243\n\t"
    "v_accvgpr_write_b32 a116, v244\n\t"
    "v_accvgpr_read_b32 v245, a117\n\t"
    "v_mul_f32 v245, v245, v243\n\t"
    "v_accvgpr_write_b32 a117, v245\n\t"
    "v_accvgpr_read_b32 v244, a118\n\t"
    "v_mul_f32 v244, v244, v243\n\t"
    "v_accvgpr_write_b32 a118, v244\n\t"
    "v_accvgpr_read_b32 v245, a119\n\t"
    "v_mul_f32 v245, v245, v243\n\t"
    "v_accvgpr_write_b32 a119, v245\n\t"
    "v_accvgpr_read_b32 v244, a120\n\t"
    "v_mul_f32 v244, v244, v243\n\t"
    "v_accvgpr_write_b32 a120, v244\n\t"
    "v_accvgpr_read_b32 v245, a121\n\t"
    "v_mul_f32 v245, v245, v243\n\t"
    "v_accvgpr_write_b32 a121, v245\n\t"
    "v_accvgpr_read_b32 v244, a122\n\t"
    "v_mul_f32 v244, v244, v243\n\t"
    "v_accvgpr_write_b32 a122, v244\n\t"
    "v_accvgpr_read_b32 v245, a123\n\t"
    "v_mul_f32 v245, v245, v243\n\t"
    "v_accvgpr_write_b32 a123, v245\n\t"
    "v_accvgpr_read_b32 v244, a124\n\t"
    "v_mul_f32 v244, v244, v243\n\t"
    "v_accvgpr_write_b32 a124, v244\n\t"
    "v_accvgpr_read_b32 v245, a125\n\t"
    "v_mul_f32 v245, v245, v243\n\t"
    "v_accvgpr_write_b32 a125, v245\n\t"
    "v_accvgpr_read_b32 v244, a126\n\t"
    "v_mul_f32 v244, v244, v243\n\t"
    "v_accvgpr_write_b32 a126, v244\n\t"
    "v_accvgpr_read_b32 v245, a127\n\t"
    "v_mul_f32 v245, v245, v243\n\t"
    "v_accvgpr_write_b32 a127, v245\n\t"
    "v_sub_f32 v244, v16, v241\n\t"
    "v_sub_f32 v245, v17, v241\n\t"
    "v_exp_f32 v244, v244\n\t"
    "v_exp_f32 v245, v245\n\t"
    "s_nop 0\n\t"
    "v_add_f32 v225, v244, v245\n\t"
    "v_cvt_pk_bf16_f32 v88, v244, v245\n\t"
    "v_sub_f32 v244, v18, v241\n\t"
    "v_sub_f32 v245, v19, v241\n\t"
    "v_exp_f32 v244, v244\n\t"
    "v_exp_f32 v245, v245\n\t"
    "s_nop 0\n\t"
    "v_add_f32 v225, v225, v244\n\t"
    "v_add_f32 v225, v225, v245\n\t"
    "v_cvt_pk_bf16_f32 v89, v244, v245\n\t"
    "v_sub_f32 v244, v20, v241\n\t"
    "v_sub_f32 v245, v21, v241\n\t"
    "v_exp_f32 v244, v244\n\t"
    "v_exp_f32 v245, v245\n\t"
    "s_nop 0\n\t"
    "v_add_f32 v225, v225, v244\n\t"
    "v_add_f32 v225, v225, v245\n\t"
    "v_cvt_pk_bf16_f32 v90, v244, v245\n\t"
    "v_sub_f32 v244, v22, v241\n\t"
    "v_sub_f32 v245, v23, v241\n\t"
    "v_exp_f32 v244, v244\n\t"
    "v_exp_f32 v245, v245\n\t"
    "s_nop 0\n\t"
    "v_add_f32 v225, v225, v244\n\t"
    "v_add_f32 v225, v225, v245\n\t"
    "v_cvt_pk_bf16_f32 v91, v244, v245\n\t"
    "v_sub_f32 v244, v24, v241\n\t"
    "v_sub_f32 v245, v25, v241\n\t"
    "v_exp_f32 v244, v244\n\t"
    "v_exp_f32 v245, v245\n\t"
    "s_nop 0\n\t"
    "v_add_f32 v225, v225, v244\n\t"
    "v_add_f32 v225, v225, v245\n\t"
    "v_cvt_pk_bf16_f32 v92, v244, v245\n\t"
    "v_sub_f32 v244, v26, v241\n\t"
    "v_sub_f32 v245, v27, v241\n\t"
    "v_exp_f32 v244, v244\n\t"
    "v_exp_f32 v245, v245\n\t"
    "s_nop 0\n\t"
    "v_add_f32 v225, v225, v244\n\t"
    "v_add_f32 v225, v225, v245\n\t"
    "v_cvt_pk_bf16_f32 v93, v244, v245\n\t"
    "v_sub_f32 v244, v28, v241\n\t"
    "v_sub_f32 v245, v29, v241\n\t"
    "v_exp_f32 v244, v244\n\t"
    "v_exp_f32 v245, v245\n\t"
    "s_nop 0\n\t"
    "v_add_f32 v225, v225, v244\n\t"
    "v_add_f32 v225, v225, v245\n\t"
    "v_cvt_pk_bf16_f32 v94, v244, v245\n\t"
    "v_sub_f32 v244, v30, v241\n\t"
    "v_sub_f32 v245, v31, v241\n\t"
    "v_exp_f32 v244, v244\n\t"
    "v_exp_f32 v245, v245\n\t"
    "s_nop 0\n\t"
    "v_add_f32 v225, v225, v244\n\t"
    "v_add_f32 v225, v225, v245\n\t"
    "v_cvt_pk_bf16_f32 v95, v244, v245\n\t"
    "s_nop 1\n\t"
    "s_branch L_ret_c1t_%=\n\t"
    "L_rare_c16t_%=:\n\t"
    "s_nop 15\n\t"
    "v_max3_f32 v241, v64, v65, v66\n\t"
    "v_max_f32 v241, v241, v67\n\t"
    "v_mov_b32 v242, v241\n\t"
    "s_nop 1\n\t"
    "v_permlane16_swap_b32 v241, v242\n\t"
    "v_max_f32 v241, v241, v242\n\t"
    "v_mov_b32 v242, v241\n\t"
    "s_nop 1\n\t"
    "v_permlane32_swap_b32 v241, v242\n\t"
    "v_max_f32 v241, v241, v242\n\t"
    "v_ceil_f32 v241, v241\n\t"
    "v_max_f32 v241, 0, v241\n\t"
    "v_sub_f32 v243, 0, v241\n\t"
    "v_exp_f32 v243, v243\n\t"
    "v_sub_f32 v244, v68, v241\n\t"
    "v_mov_b32 v68, v244\n\t"
    "v_mov_b32 v69, v244\n\t"
    "v_mov_b32 v70, v244\n\t"
    "v_mov_b32 v71, v244\n\t"
    "v_mul_f32 v229, v229, v243\n\t"
    "v_accvgpr_read_b32 v244, a128\n\t"
    "v_mul_f32 v244, v244, v243\n\t"
    "v_accvgpr_write_b32 a128, v244\n\t"
    "v_accvgpr_read_b32 v245, a129\n\t"
    "v_mul_f32 v245, v245, v243\n\t"
    "v_accvgpr_write_b32 a129, v245\n\t"
    "v_accvgpr_read_b32 v244, a130\n\t"
    "v_mul_f32 v244, v244, v243\n\t"
    "v_accvgpr_write_b32 a130, v244\n\t"
    "v_accvgpr_read_b32 v245, a131\n\t"
    "v_mul_f32 v245, v245, v243\n\t"
    "v_accvgpr_write_b32 a131, v245\n\t"
    "v_accvgpr_read_b32 v244, a132\n\t"
    "v_mul_f32 v244, v244, v243\n\t"
    "v_accvgpr_write_b32 a132, v244\n\t"
    "v_accvgpr_read_b32 v245, a133\n\t"
    "v_mul_f32 v245, v245, v243\n\t"
    "v_accvgpr_write_b32 a133, v245\n\t"
    "v_accvgpr_read_b32 v244, a134\n\t"
    "v_mul_f32 v244, v244, v243\n\t"
    "v_accvgpr_write_b32 a134, v244\n\t"
    "v_accvgpr_read_b32 v245, a135\n\t"
    "v_mul_f32 v245, v245, v243\n\t"
    "v_accvgpr_write_b32 a135, v245\n\t"
    "v_accvgpr_read_b32 v244, a136\n\t"
    "v_mul_f32 v244, v244, v243\n\t"
    "v_accvgpr_write_b32 a136, v244\n\t"
    "v_accvgpr_read_b32 v245, a137\n\t"
    "v_mul_f32 v245, v245, v243\n\t"
    "v_accvgpr_write_b32 a137, v245\n\t"
    "v_accvgpr_read_b32 v244, a138\n\t"
    "v_mul_f32 v244, v244, v243\n\t"
    "v_accvgpr_write_b32 a138, v244\n\t"
    "v_accvgpr_read_b32 v245, a139\n\t"
    "v_mul_f32 v245, v245, v243\n\t"
    "v_accvgpr_write_b32 a139, v245\n\t"
    "v_accvgpr_read_b32 v244, a140\n\t"
    "v_mul_f32 v244, v244, v243\n\t"
    "v_accvgpr_write_b32 a140, v244\n\t"
    "v_accvgpr_read_b32 v245, a141\n\t"
    "v_mul_f32 v245, v245, v243\n\t"
    "v_accvgpr_write_b32 a141, v245\n\t"
    "v_accvgpr_read_b32 v244, a142\n\t"
    "v_mul_f32 v244, v244, v243\n\t"
    "v_accvgpr_write_b32 a142, v244\n\t"
    "v_accvgpr_read_b32 v245, a143\n\t"
    "v_mul_f32 v245, v245, v243\n\t"
    "v_accvgpr_write_b32 a143, v245\n\t"
    "v_accvgpr_read_b32 v244, a144\n\t"
    "v_mul_f32 v244, v244, v243\n\t"
    "v_accvgpr_write_b32 a144, v244\n\t"
    "v_accvgpr_read_b32 v245, a145\n\t"
    "v_mul_f32 v245, v245, v243\n\t"
    "v_accvgpr_write_b32 a145, v245\n\t"
    "v_accvgpr_read_b32 v244, a146\n\t"
    "v_mul_f32 v244, v244, v243\n\t"
    "v_accvgpr_write_b32 a146, v244\n\t"
    "v_accvgpr_read_b32 v245, a147\n\t"
    "v_mul_f32 v245, v245, v243\n\t"
    "v_accvgpr_write_b32 a147, v245\n\t"
    "v_accvgpr_read_b32 v244, a148\n\t"
    "v_mul_f32 v244, v244, v243\n\t"
    "v_accvgpr_write_b32 a148, v244\n\t"
    "v_accvgpr_read_b32 v245, a149\n\t"
    "v_mul_f32 v245, v245, v243\n\t"
    "v_accvgpr_write_b32 a149, v245\n\t"
    "v_accvgpr_read_b32 v244, a150\n\t"
    "v_mul_f32 v244, v244, v243\n\t"
    "v_accvgpr_write_b32 a150, v244\n\t"
    "v_accvgpr_read_b32 v245, a151\n\t"
    "v_mul_f32 v245, v245, v243\n\t"
    "v_accvgpr_write_b32 a151, v245\n\t"
    "v_accvgpr_read_b32 v244, a152\n\t"
    "v_mul_f32 v244, v244, v243\n\t"
    "v_accvgpr_write_b32 a152, v244\n\t"
    "v_accvgpr_read_b32 v245, a153\n\t"
    "v_mul_f32 v245, v245, v243\n\t"
    "v_accvgpr_write_b32 a153, v245\n\t"
    "v_accvgpr_read_b32 v244, a154\n\t"
    "v_mul_f32 v244, v244, v243\n\t"
    "v_accvgpr_write_b32 a154, v244\n\t"
    "v_accvgpr_read_b32 v245, a155\n\t"
    "v_mul_f32 v245, v245, v243\n\t"
    "v_accvgpr_write_b32 a155, v245\n\t"
    "v_accvgpr_read_b32 v244, a156\n\t"
    "v_mul_f32 v244, v244, v243\n\t"
    "v_accvgpr_write_b32 a156, v244\n\t"
    "v_accvgpr_read_b32 v245, a157\n\t"
    "v_mul_f32 v245, v245, v243\n\t"
    "v_accvgpr_write_b32 a157, v245\n\t"
    "v_accvgpr_read_b32 v244, a158\n\t"
    "v_mul_f32 v244, v244, v243\n\t"
    "v_accvgpr_write_b32 a158, v244\n\t"
    "v_accvgpr_read_b32 v245, a159\n\t"
    "v_mul_f32 v245, v245, v243\n\t"
    "v_accvgpr_write_b32 a159, v245\n\t"
    "v_lshlrev_b32 v244, 16, v96\n\t"
    "v_and_b32 v245, 0xffff0000, v96\n\t"
    "v_mul_f32 v244, v244, v243\n\t"
    "v_mul_f32 v245, v245, v243\n\t"
    "v_cvt_pk_bf16_f32 v96, v244, v245\n\t"
    "v_lshlrev_b32 v244, 16, v97\n\t"
    "v_and_b32 v245, 0xffff0000, v97\n\t"
    "v_mul_f32 v244, v244, v243\n\t"
    "v_mul_f32 v245, v245, v243\n\t"
    "v_cvt_pk_bf16_f32 v97, v244, v245\n\t"
    "v_sub_f32 v244, v64, v241\n\t"
    "v_sub_f32 v245, v65, v241\n\t"
    "v_exp_f32 v244, v244\n\t"
    "v_exp_f32 v245, v245\n\t"
    "s_nop 0\n\t"
    "v_add_f32 v226, v244, v245\n\t"
    "v_cvt_pk_bf16_f32 v98, v244, v245\n\t"
    "v_sub_f32 v244, v66, v241\n\t"
    "v_sub_f32 v245, v67, v241\n\t"
    "v_exp_f32 v244, v244\n\t"
    "v_exp_f32 v245, v245\n\t"
    "s_nop 0\n\t"
    "v_add_f32 v226, v226, v244\n\t"
    "v_add_f32 v226, v226, v245\n\t"
    "v_cvt_pk_bf16_f32 v99, v244, v245\n\t"
    "s_nop 1\n\t"
    "s_branch L_ret_c16t_%=\n\t"
    :: "s"(__builtin_amdgcn_kernarg_segment_ptr()), "s"(blockIdx.x), "s"(w), "s"((unsigned)(size_t)lds)
    : "memory", "vcc", "scc", "v0", "v1", "v2", "v3", "v4", "v5", "v6", "v7", "v8", "v9", "v10", "v11", "v12", "v13", "v14", "v15", "v16", "v17", "v18", "v19", "v20", "v21", "v22", "v23", "v24", "v25", "v26", "v27", "v28", "v29", "v30", "v31", "v32", "v33", "v34", "v35", "v36", "v37", "v38", "v39", "v40", "v41", "v42", "v43", "v44", "v45", "v46", "v47", "v48", "v49", "v50", "v51", "v52", "v53", "v54", "v55", "v56", "v57", "v58", "v59", "v60", "v61", "v62", "v63", "v64", "v65", "v66", "v67", "v68", "v69", "v70", "v71", "v72", "v73", "v74", "v75", "v76", "v77", "v78", "v79", "v80", "v81", "v82", "v83", "v84", "v85", "v86", "v87", "v88", "v89", "v90", "v91", "v92", "v93", "v94", "v95", "v96", "v97", "v98", "v99", "v100", "v101", "v102", "v103", "v104", "v105", "v106", "v107", "v108", "v109", "v110", "v111", "v112", "v113", "v114", "v115", "v116", "v117", "v118", "v119", "v120", "v121", "v122", "v123", "v124", "v125", "v126", "v127", "v128", "v129", "v130", "v131", "v132", "v133", "v134", "v135", "v136", "v137", "v138", "v139", "v140", "v141", "v142", "v143", "v144", "v145", "v146", "v147", "v148", "v149", "v150", "v151", "v152", "v153", "v154", "v155", "v156", "v157", "v158", "v159", "v160", "v161", "v162", "v163", "v164", "v165", "v166", "v167", "v168", "v169", "v170", "v171", "v172", "v173", "v174", "v175", "v176", "v177", "v178", "v179", "v180", "v181", "v182", "v183", "v184", "v185", "v186", "v187", "v188", "v189", "v190", "v191", "v192", "v193", "v194", "v195", "v196", "v197", "v198", "v199", "v200", "v201", "v202", "v203", "v204", "v205", "v206", "v207", "v208", "v209", "v210", "v211", "v212", "v213", "v214", "v215", "v216", "v217", "v218", "v219", "v220", "v221", "v222", "v223", "v224", "v225", "v226", "v227", "v228", "v229", "v230", "v231", "v232", "v233", "v234", "v235", "v236", "v237", "v238", "v239", "v240", "v241", "v242", "v243", "v244", "v245", "v246", "v247", "v248", "v249", "v250", "v251", "v252", "v253", "v254", "v255", "a0", "a1", "a2", "a3", "a4", "a5", "a6", "a7", "a8", "a9", "a10", "a11", "a12", "a13", "a14", "a15", "a16", "a17", "a18", "a19", "a20", "a21", "a22", "a23", "a24", "a25", "a26", "a27", "a28", "a29", "a30", "a31", "a32", "a33", "a34", "a35", "a36", "a37", "a38", "a39", "a40", "a41", "a42", "a43", "a44", "a45", "a46", "a47", "a48", "a49", "a50", "a51", "a52", "a53", "a54", "a55", "a56", "a57", "a58", "a59", "a60", "a61", "a62", "a63", "a64", "a65", "a66", "a67", "a68", "a69", "a70", "a71", "a72", "a73", "a74", "a75", "a76", "a77", "a78", "a79", "a80", "a81", "a82", "a83", "a84", "a85", "a86", "a87", "a88", "a89", "a90", "a91", "a92", "a93", "a94", "a95", "a96", "a97", "a98", "a99", "a100", "a101", "a102", "a103", "a104", "a105", "a106", "a107", "a108", "a109", "a110", "a111", "a112", "a113", "a114", "a115", "a116", "a117", "a118", "a119", "a120", "a121", "a122", "a123", "a124", "a125", "a126", "a127", "a128", "a129", "a130", "a131", "a132", "a133", "a134", "a135", "a136", "a137", "a138", "a139", "a140", "a141", "a142", "a143", "a144", "a145", "a146", "a147", "a148", "a149", "a150", "a151", "a152", "a153", "a154", "a155", "a156", "a157", "a158", "a159", "a160", "a161", "a162", "a163", "a164", "a165", "a166", "a167", "a168", "a169", "a170", "a171", "a172", "a173", "a174", "a175", "a176", "a177", "a178", "a179", "a180", "a181", "a182", "a183", "a184", "a185", "a186", "a187", "a188", "a189", "a190", "a191", "a192", "a193", "a194", "a195", "a196", "a197", "a198", "a199", "a200", "a201", "a202", "a203", "a204", "a205", "a206", "a207", "a208", "a209", "a210", "a211", "a212", "a213", "a214", "a215", "a216", "a217", "a218", "a219", "a220", "a221", "a222", "a223", "a224", "a225", "a226", "a227", "a228", "a229", "a230", "a231", "a232", "a233", "a234", "a235", "a236", "a237", "a238", "a239", "a240", "a241", "a242", "a243", "a244", "a245", "a246", "a247", "a248", "a249", "a250", "a251", "a252", "a253", "a254", "a255", "s36", "s37", "s38", "s39", "s40", "s41", "s42", "s43", "s44", "s45", "s46", "s47", "s48", "s49", "s50", "s51", "s52", "s53", "s54", "s55", "s56", "s57", "s58", "s59", "s60", "s61", "s62", "s63", "s64", "s65", "s66", "s67", "s68", "s69", "s70", "s71", "s72", "s73", "s74", "s75", "s76", "s77", "s78", "s79", "s80", "s81", "s82", "s83", "s84", "s85", "s86", "s87", "s88", "s89", "s90", "s91", "s92", "s93", "s94", "s95", "s96", "s97", "s98", "s99");
}

extern "C" void kernel_launch(void* const* d_in, const int* in_sizes, int n_in, void* d_out, int out_size, void* d_ws, size_t ws_size, hipStream_t stream) {
  gqa_attn_fwd<<<256, 256, 0, stream>>>(d_in[0], d_in[1], d_in[2], d_out);
}
```
